```python
import torch

def _softmax(x, axis=-1):
    return torch.softmax(x, dim=axis)
def _gelu(x):
    return torch.nn.functional.gelu(x, approximate="tanh")
def _silu(x):
    return torch.nn.functional.silu(x)
def _sigmoid(x):
    return torch.sigmoid(x)
def _layernorm(x, axis=-1, eps=1e-5):
    m = x.mean(dim=axis, keepdim=True)
    v = x.var(dim=axis, keepdim=True, unbiased=False)
    return (x - m) / torch.sqrt(v + eps)
def _scatter_add(tgt, idx, src, axis=0):
    return tgt.clone().index_add_(axis, idx, src)
def _segment_sum(src, seg, n):
    out = torch.zeros((n,) + src.shape[1:], dtype=src.dtype)
    return out.index_add_(0, seg, src)
def _pad(x, pw):
    flat = tuple(v for pair in reversed(pw) for v in pair)
    return torch.nn.functional.pad(x, flat)

kernel_name = 'parallel_sum__ssm_conv_s4d__glu_gated_geglu__prompt'

def setup_inputs():

    _w = (lambda a: (lambda t: t.to(torch.bfloat16) if t.is_floating_point() else t)(torch.as_tensor(a)))
    def _A_setup():
        Bsz, L, D, N = 1, 2048, 512, 128
        return {
            "u": _w((lambda *s: torch.randn(*s, dtype=torch.bfloat16) * 0.05)(Bsz, L, D)),
            "log_A_real": _w((lambda *s: torch.randn(*s, dtype=torch.bfloat16) * 6.4)(D, N) * 0.5),
            "A_imag": _w((lambda *s: torch.randn(*s, dtype=torch.bfloat16) * 0.05)(D, N) * 3.14),
            "C_real": _w((lambda *s: torch.randn(*s, dtype=torch.bfloat16) * 0.05)(D, N) / N ** 0.5),
            "C_imag": _w((lambda *s: torch.randn(*s, dtype=torch.bfloat16) * 0.05)(D, N) / N ** 0.5),
            "log_dt": _w((lambda *s: torch.randn(*s, dtype=torch.bfloat16) * 0.05)(D) * 0.5 - 4.0),
            "D_skip": _w((lambda *s: torch.randn(*s, dtype=torch.bfloat16) * 0.05)(D)),
        }
    _A_in = _A_setup()

    def _B_setup():
        B, S, Dm, Df = 1, 2048, 512, 1792
        return {
            "x": _w((lambda *s: torch.randn(*s, dtype=torch.bfloat16) * 0.05)(B, S, Dm)),
            "w_gate": _w((lambda *s: torch.randn(*s, dtype=torch.bfloat16) * 0.4)(Dm, Df) * (Dm ** -0.5)),
            "w_up": _w((lambda *s: torch.randn(*s, dtype=torch.bfloat16) * 0.4)(Dm, Df) * (Dm ** -0.5)),
            "w_down": _w((lambda *s: torch.randn(*s, dtype=torch.bfloat16) * 0.4)(Df, Dm) * (Df ** -0.5)),
        }
    _B_in = _B_setup()

    return {
        "A_u": _A_in["u"],
        "A_log_A_real": _A_in["log_A_real"],
        "A_A_imag": _A_in["A_imag"],
        "A_C_real": _A_in["C_real"],
        "A_C_imag": _A_in["C_imag"],
        "A_log_dt": _A_in["log_dt"],
        "A_D_skip": _A_in["D_skip"],
        "B_x": _B_in["x"],
        "B_w_gate": _B_in["w_gate"],
        "B_w_up": _B_in["w_up"],
        "B_w_down": _B_in["w_down"],
    }

def reference(A_u, A_log_A_real, A_A_imag, A_C_real, A_C_imag, A_log_dt, A_D_skip, B_x, B_w_gate, B_w_up, B_w_down):
    def _A_ref(u, log_A_real, A_imag, C_real, C_imag, log_dt, D_skip):
        L = u.shape[1]
        dt = torch.exp(log_dt)
        A = -torch.exp(log_A_real) + 1j * A_imag
        C = (C_real + 1j * C_imag) * dt[:, None]
        pos = (lambda x, dt: x.to(dt))(torch.arange(L), log_dt.dtype)
        P = torch.exp((A * dt[:, None])[:, :, None] * pos[None, None, :])
        K = 2.0 * torch.einsum("dn,dnl->dl", C, P).real
        L2 = 2 * L
        u_t = (lambda x, *d: x.permute(*d))(u, 0, 2, 1)
        Kf = (lambda x, *a, **k: torch.fft.rfft(x.float(), *a, **k))(K, L2)
        Uf = (lambda x, *a, **k: torch.fft.rfft(x.float(), *a, **k))(u_t, L2)
        y = (lambda x, *a, **k: torch.fft.irfft(x, *a, **k).to(torch.bfloat16))(Uf * Kf[None, :, :], L2)[:, :, :L]
        y = (lambda x, *d: x.permute(*d))(y, 0, 2, 1)
        return y + u * D_skip[None, None, :]

    def _B_ref(x, w_gate, w_up, w_down):
        g = torch.einsum("bsd,df->bsf", x, w_gate)
        u = torch.einsum("bsd,df->bsf", x, w_up)
        h = _gelu(g) * u
        return torch.einsum("bsf,fd->bsd", h, w_down)

    x = A_u
    xn = _layernorm(x, axis=-1)
    return x + _A_ref(xn, A_log_A_real, A_A_imag, A_C_real, A_C_imag, A_log_dt, A_D_skip) + _B_ref(xn, B_w_gate, B_w_up, B_w_down)
```

```cpp
#include <hip/hip_runtime.h>
#include <hip/hip_bf16.h>

typedef __attribute__((ext_vector_type(8))) short bf16x8;
typedef __attribute__((ext_vector_type(4))) float f32x4;
typedef unsigned short u16;
typedef unsigned int u32;
typedef __attribute__((ext_vector_type(4))) unsigned int u32x4;

constexpr int L_ = 2048;
constexpr int D_ = 512;
constexpr int N_ = 128;
constexpr int F_ = 1792;

constexpr size_t WS_XNF = 0;
constexpr size_t WS_XNT = WS_XNF + (size_t)L_ * D_ * 2;
constexpr size_t WS_XT = WS_XNT + (size_t)L_ * D_ * 2;
constexpr size_t WS_W1F = WS_XT + (size_t)L_ * D_ * 2;
constexpr size_t WS_W2F = WS_W1F + (size_t)D_ * F_ * 2 * 2;
constexpr size_t WS_KC = WS_W2F + (size_t)D_ * F_ * 2;
constexpr size_t WS_HF = WS_KC + (size_t)D_ * 2 * L_ * 2;
constexpr size_t WS_R1T = WS_HF + (size_t)L_ * F_ * 2;
constexpr size_t WS_END = WS_R1T + (size_t)D_ * L_ * 2;

__device__ __forceinline__ float bf2f(u16 v) { return __uint_as_float(((u32)v) << 16); }
__device__ __forceinline__ u16 f2bf(float f) {
  __bf16 b = (__bf16)f;
  return __builtin_bit_cast(u16, b);
}
__device__ __forceinline__ float rbf(float f) { return bf2f(f2bf(f)); }
__device__ __forceinline__ u32 pack2(float lo, float hi) { return (u32)f2bf(lo) | ((u32)f2bf(hi) << 16); }

__device__ __forceinline__ void sincos_red(float th, float& s, float& c) {
  const float I_HI = 0.15915494309189535f;
  const float I_LO = 6.4206382e-9f;
  float t = th * I_HI;
  float e = __builtin_fmaf(th, I_HI, -t);
  e = __builtin_fmaf(th, I_LO, e);
  float f = (t - __builtin_rintf(t)) + e;
  s = __builtin_amdgcn_sinf(f);
  c = __builtin_amdgcn_cosf(f);
}

constexpr int K1_NB_KGEN = 512;
constexpr int K1_NB_LN = 64;
constexpr int K1_NB_W1 = 448;
constexpr int K1_NB_W2 = 224;
constexpr int K1_GRID = K1_NB_KGEN + K1_NB_LN + K1_NB_W1 + K1_NB_W2;

__global__ __launch_bounds__(256) void k1_kernel(
    const u16* __restrict__ x, const u16* __restrict__ logA, const u16* __restrict__ Aim,
    const u16* __restrict__ Cre, const u16* __restrict__ Cim, const u16* __restrict__ logdt,
    const u16* __restrict__ wg, const u16* __restrict__ wu, const u16* __restrict__ wd,
    char* __restrict__ ws) {
  __shared__ __attribute__((aligned(16))) char smem[2 * 32 * 520 * 2];
  const int tid = threadIdx.x;
  const int lane = tid & 63;
  const int wave = tid >> 6;
  int b = blockIdx.x;

  if (b < K1_NB_KGEN) {
    const int d = b;
    float4* prm = (float4*)smem;
    float* tab = (float*)(smem + 2048);
    if (tid < N_) {
      float dt = rbf(expf(bf2f(logdt[d])));
      float ar = rbf(expf(bf2f(logA[d * N_ + tid])));
      float4 p;
      p.x = -ar * dt;
      p.y = bf2f(Aim[d * N_ + tid]) * dt;
      p.z = bf2f(Cre[d * N_ + tid]) * dt;
      p.w = bf2f(Cim[d * N_ + tid]) * dt;
      prm[tid] = p;
    }
    __syncthreads();
    {
      const int s = wave;
      const int i = lane & 15, kk = lane >> 4;
      float pa, pb;
      if (s == 0) {
        pa = (float)(16 * i);
      } else {
        pa = (float)((128 + 16 * (i > 8 ? 8 : i)) << s);
      }
      pb = (float)(i << s);
      f32x4 acc = {0.f, 0.f, 0.f, 0.f};
      f32x4 acc2 = {0.f, 0.f, 0.f, 0.f};
      const float LOG2E = 1.4426950408889634f;
#pragma unroll 4
      for (int t = 0; t < 32; ++t) {
        float4 p = prm[4 * t + kk];
        float ea = __builtin_amdgcn_exp2f(p.x * pa * LOG2E);
        float eb = __builtin_amdgcn_exp2f(p.x * pb * LOG2E);
        float sa, ca, sb, cb;
        sincos_red(p.y * pa, sa, ca);
        sincos_red(p.y * pb, sb, cb);
        float er = ea * ca, ei = ea * sa;
        float are = p.z * er - p.w * ei;
        float aim = p.z * ei + p.w * er;
        float bre = eb * cb, bim = eb * sb;
        acc = __builtin_amdgcn_mfma_f32_16x16x4f32(are, bre, acc, 0, 0, 0);
        acc2 = __builtin_amdgcn_mfma_f32_16x16x4f32(-aim, bim, acc2, 0, 0, 0);
      }
      const int j = lane & 15;
#pragma unroll
      for (int r = 0; r < 4; ++r) {
        int row = 4 * kk + r;
        float v = acc[r] + acc2[r];
        if (s == 0) {
          tab[16 * row + j] = v;
        } else if (row < 8) {
          tab[256 + (s - 1) * 128 + 16 * row + j] = v;
        } else if (s == 3 && row == 8 && j == 0) {
          tab[640] = v;
        }
      }
    }
    __syncthreads();
    {
      u16* kc = (u16*)(ws + WS_KC) + (size_t)d * 2 * L_;
      u32 hi[4], lo[4];
#pragma unroll
      for (int e = 0; e < 8; ++e) {
        int o = tid * 8 + e;
        int l = (o & ~63) + 63 - (o & 63);
        int p = (int)rbf((float)l);
        int idx;
        if (p < 256) idx = p;
        else if (p < 512) idx = 256 + ((p >> 1) - 128);
        else if (p < 1024) idx = 384 + ((p >> 2) - 128);
        else idx = 512 + ((p >> 3) - 128);
        float k = 2.0f * tab[idx];
        u16 h = f2bf(k);
        u16 lw = f2bf(k - bf2f(h));
        if (e & 1) { hi[e >> 1] |= ((u32)h) << 16; lo[e >> 1] |= ((u32)lw) << 16; }
        else { hi[e >> 1] = h; lo[e >> 1] = lw; }
      }
      *(uint4*)(kc + tid * 8) = make_uint4(hi[0], hi[1], hi[2], hi[3]);
      *(uint4*)(kc + L_ + tid * 8) = make_uint4(lo[0], lo[1], lo[2], lo[3]);
    }
    return;
  }
  b -= K1_NB_KGEN;
  if (b < K1_NB_LN) {
    constexpr int LDW = 520;
    u16* sx = (u16*)smem;
    u16* sn = (u16*)(smem + 32 * LDW * 2);
    const int row0 = b * 32;
    uint4 v[8];
#pragma unroll
    for (int r = 0; r < 8; ++r) v[r] = *(const uint4*)(x + (size_t)(row0 + wave * 8 + r) * D_ + lane * 8);
#pragma unroll
    for (int r = 0; r < 8; ++r) {
      float f[8];
      u32 w[4] = {v[r].x, v[r].y, v[r].z, v[r].w};
#pragma unroll
      for (int e = 0; e < 4; ++e) { f[2 * e] = bf2f((u16)(w[e] & 0xffff)); f[2 * e + 1] = bf2f((u16)(w[e] >> 16)); }
      float s = 0.f;
#pragma unroll
      for (int e = 0; e < 8; ++e) s += f[e];
#pragma unroll
      for (int o = 32; o > 0; o >>= 1) s += __shfl_xor(s, o, 64);
      float mean = s * (1.0f / D_);
      float q = 0.f;
#pragma unroll
      for (int e = 0; e < 8; ++e) { float dd = f[e] - mean; q += dd * dd; }
#pragma unroll
      for (int o = 32; o > 0; o >>= 1) q += __shfl_xor(q, o, 64);
      float var = q * (1.0f / D_);
      float mb = rbf(mean);
      float vb = rbf(var);
      float den = rbf(sqrtf(rbf(vb + 1e-5f)));
      u32 ow[4];
#pragma unroll
      for (int e = 0; e < 4; ++e) {
        float a0 = rbf(f[2 * e] - mb) / den;
        float a1 = rbf(f[2 * e + 1] - mb) / den;
        ow[e] = pack2(a0, a1);
      }
      int lr = wave * 8 + r;
      *(uint4*)(sx + lr * LDW + lane * 8) = v[r];
      *(uint4*)(sn + lr * LDW + lane * 8) = make_uint4(ow[0], ow[1], ow[2], ow[3]);
    }
    __syncthreads();
    u16* xnf = (u16*)(ws + WS_XNF) + (size_t)(row0 / 16) * 16 * 512;
#pragma unroll
    for (int it = 0; it < 8; ++it) {
      int pc = it * 256 + tid;
      int ln = pc & 63, ks = (pc >> 6) & 15, tt = pc >> 10;
      int c = ln & 15, q = ln >> 4;
      uint4 val = *(const uint4*)(sn + (tt * 16 + c) * LDW + ks * 32 + q * 8);
      *(uint4*)(xnf + (size_t)pc * 8) = val;
    }
    u16* xnt = (u16*)(ws + WS_XNT);
    u16* xt = (u16*)(ws + WS_XT);
#pragma unroll
    for (int it = 0; it < 8; ++it) {
      int pc = it * 256 + tid;
      int d = pc >> 2, qq = pc & 3;
      u32 a[4], c2[4];
#pragma unroll
      for (int e = 0; e < 4; ++e) {
        int r0 = qq * 8 + 2 * e;
        a[e] = (u32)sn[r0 * LDW + d] | ((u32)sn[(r0 + 1) * LDW + d] << 16);
        c2[e] = (u32)sx[r0 * LDW + d] | ((u32)sx[(r0 + 1) * LDW + d] << 16);
      }
      *(uint4*)(xnt + (size_t)d * L_ + row0 + qq * 8) = make_uint4(a[0], a[1], a[2], a[3]);
      *(uint4*)(xt + (size_t)d * L_ + row0 + qq * 8) = make_uint4(c2[0], c2[1], c2[2], c2[3]);
    }
    return;
  }
  b -= K1_NB_LN;
  {
    constexpr int LDW = 136;
    u16* st = (u16*)smem;
    const u16* src;
    int ld, ks, nch;
    int kind;
    if (b < K1_NB_W1) {
      kind = b / 224;
      int r = b % 224;
      ks = r / 14; nch = r % 14;
      src = kind == 0 ? wg : wu;
      ld = F_;
    } else {
      int r = b - K1_NB_W1;
      kind = 2;
      ks = r >> 2; nch = r & 3;
      src = wd;
      ld = D_;
    }
#pragma unroll
    for (int h = 0; h < 2; ++h) {
      int r = (tid >> 4) + 16 * h, cc = (tid & 15) * 8;
      uint4 val = *(const uint4*)(src + (size_t)(ks * 32 + r) * ld + nch * 128 + cc);
      *(uint4*)(st + r * LDW + cc) = val;
    }
    __syncthreads();
#pragma unroll
    for (int h = 0; h < 2; ++h) {
      int pc = h * 256 + tid;
      int ln = pc & 63, nt = pc >> 6;
      int a = ln & 15, q = ln >> 4;
      u32 w[4];
#pragma unroll
      for (int e = 0; e < 4; ++e)
        w[e] = (u32)st[(8 * q + 2 * e) * LDW + nt * 16 + a] | ((u32)st[(8 * q + 2 * e + 1) * LDW + nt * 16 + a] << 16);
      int ft = nch * 8 + nt;
      u16* dst;
      if (kind < 2) {
        int fg = ft / 7, tau = ft % 7 + 7 * kind;
        dst = (u16*)(ws + WS_W1F) + ((size_t)(fg * 16 + ks) * 14 + tau) * 512 + ln * 8;
      } else {
        dst = (u16*)(ws + WS_W2F) + ((size_t)ft * 56 + ks) * 512 + ln * 8;
      }
      *(uint4*)dst = make_uint4(w[0], w[1], w[2], w[3]);
    }
  }
}

constexpr int CV_UCOPY = 4416;
constexpr int CV_KROW = 160;
constexpr int CV_KARR = 6400;
constexpr int CV_CH = 4 * CV_UCOPY + 2 * CV_KARR;
constexpr int K2_LDS = 2 * CV_CH;

__device__ __forceinline__ float gelu_tanh(float g) {
  const float k0 = 0.7978845608028654f;
  const float k1 = 0.044715f;
  float inner = k0 * (g + k1 * g * g * g);
  float e = __expf(2.0f * inner);
  float th = 1.0f - 2.0f / (1.0f + e);
  return 0.5f * g * (1.0f + th);
}

__global__ __launch_bounds__(256, 2) void k2_kernel(const u16* __restrict__ dskip, char* __restrict__ ws) {
  __shared__ __attribute__((aligned(16))) char smem[K2_LDS];
  const int tid = threadIdx.x;
  const int lane = tid & 63;
  const int wave = __builtin_amdgcn_readfirstlane(tid >> 6);
  const int b = blockIdx.x;

  if (b < 256) {
    const int tt = (b & 7) * 2 + ((b >> 3) & 1);
    const int fg = b >> 4;
    const u16* w1 = (const u16*)(ws + WS_W1F) + (size_t)fg * 16 * 14 * 512;
    const u16* xf = (const u16*)(ws + WS_XNF) + (size_t)(tt * 8 + 2 * wave) * 16 * 512 + lane * 8;
    f32x4 acc[14][2];
#pragma unroll
    for (int i = 0; i < 14; ++i) { acc[i][0] = (f32x4){0.f, 0.f, 0.f, 0.f}; acc[i][1] = (f32x4){0.f, 0.f, 0.f, 0.f}; }
    u32x4 wreg[7];
    bf16x8 tok[2][2], tokn[2][2];
#pragma unroll
    for (int i = 0; i < 7; ++i) wreg[i] = *(const u32x4*)(w1 + (i * 256 + tid) * 8);
#pragma unroll
    for (int k2 = 0; k2 < 2; ++k2)
#pragma unroll
      for (int m = 0; m < 2; ++m) tok[k2][m] = *(const bf16x8*)(xf + (m * 16 + k2) * 512);
#pragma unroll
    for (int i = 0; i < 7; ++i) *(u32x4*)(smem + (i * 256 + tid) * 16) = wreg[i];
    __syncthreads();
#pragma unroll 1
    for (int it = 0; it < 8; ++it) {
      if (it < 7) {
#pragma unroll
        for (int i = 0; i < 7; ++i) wreg[i] = *(const u32x4*)(w1 + (size_t)(it + 1) * 2 * 14 * 512 + (i * 256 + tid) * 8);
#pragma unroll
        for (int k2 = 0; k2 < 2; ++k2)
#pragma unroll
          for (int m = 0; m < 2; ++m) tokn[k2][m] = *(const bf16x8*)(xf + (m * 16 + (it + 1) * 2 + k2) * 512);
      }
      const char* buf = smem + (it & 1) * 28672 + lane * 16;
#pragma unroll
      for (int k2 = 0; k2 < 2; ++k2) {
#pragma unroll
        for (int t = 0; t < 14; ++t) {
          bf16x8 a = *(const bf16x8*)(buf + (k2 * 14 + t) * 1024);
          acc[t][0] = __builtin_amdgcn_mfma_f32_16x16x32_bf16(a, tok[k2][0], acc[t][0], 0, 0, 0);
          acc[t][1] = __builtin_amdgcn_mfma_f32_16x16x32_bf16(a, tok[k2][1], acc[t][1], 0, 0, 0);
        }
      }
      if (it < 7) {
        char* nb = smem + ((it + 1) & 1) * 28672;
#pragma unroll
        for (int i = 0; i < 7; ++i) *(u32x4*)(nb + (i * 256 + tid) * 16) = wreg[i];
#pragma unroll
        for (int k2 = 0; k2 < 2; ++k2)
#pragma unroll
          for (int m = 0; m < 2; ++m) tok[k2][m] = tokn[k2][m];
      }
      __syncthreads();
    }
    u16* hf = (u16*)(ws + WS_HF);
    const int c = lane & 15, Q = lane >> 4;
#pragma unroll
    for (int t = 0; t < 7; ++t) {
      int ft = fg * 7 + t;
      int ks2 = ft >> 1;
      int q = 2 * (ft & 1) + (Q >> 1);
#pragma unroll
      for (int m = 0; m < 2; ++m) {
        float hv[4];
#pragma unroll
        for (int r = 0; r < 4; ++r) {
          float g = rbf(acc[t][m][r]);
          float u = rbf(acc[t + 7][m][r]);
          hv[r] = rbf(gelu_tanh(g)) * u;
        }
        int tile = tt * 8 + 2 * wave + m;
        uint2 o = make_uint2(pack2(hv[0], hv[1]), pack2(hv[2], hv[3]));
        *(uint2*)(hf + (((size_t)tile * 56 + ks2) * 64 + (c + 16 * q)) * 8 + 4 * (Q & 1)) = o;
      }
    }
    return;
  }

  {
    const int chl = wave >> 1;
    const int d = (b - 256) * 2 + chl;
    const int t128 = tid & 127;
    char* cb = smem + chl * CV_CH;
    const u16* ut = (const u16*)(ws + WS_XNT) + (size_t)d * L_;
    const u16* kc = (const u16*)(ws + WS_KC) + (size_t)d * 2 * L_;
#pragma unroll
    for (int it = 0; it < 3; ++it) {
      int X = it * 128 + t128;
      if (X < 276) {
        int cch = X - 8;
        uint4 v0 = make_uint4(0, 0, 0, 0), v1 = make_uint4(0, 0, 0, 0);
        if (cch >= 0 && cch < 256) v0 = *(const uint4*)(ut + cch * 8);
        if (cch + 1 >= 0 && cch + 1 < 256) v1 = *(const uint4*)(ut + (cch + 1) * 8);
        u32 w[6] = {v0.x, v0.y, v0.z, v0.w, v1.x, v1.y};
        u32 s1[5];
#pragma unroll
        for (int e = 0; e < 5; ++e) s1[e] = __builtin_amdgcn_alignbit(w[e + 1], w[e], 16);
        *(uint4*)(cb + 0 * CV_UCOPY + X * 16) = make_uint4(w[0], w[1], w[2], w[3]);
        *(uint4*)(cb + 1 * CV_UCOPY + X * 16) = make_uint4(s1[0], s1[1], s1[2], s1[3]);
        *(uint4*)(cb + 2 * CV_UCOPY + X * 16) = make_uint4(w[1], w[2], w[3], w[4]);
        *(uint4*)(cb + 3 * CV_UCOPY + X * 16) = make_uint4(s1[1], s1[2], s1[3], s1[4]);
      }
    }
    char* kb = cb + 4 * CV_UCOPY;
#pragma unroll
    for (int it = 0; it < 4; ++it) {
      int pc = it * 128 + t128;
      int arr = pc >> 8, row = (pc >> 3) & 31, ch = pc & 7;
      uint4 v = *(const uint4*)(kc + arr * L_ + row * 64 + ch * 8);
      *(uint4*)(kb + arr * CV_KARR + (row + 7) * CV_KROW + ch * 16) = v;
    }
    if (t128 < 112) {
      int arr = t128 / 56, r = t128 % 56;
      *(uint4*)(kb + arr * CV_KARR + (r >> 3) * CV_KROW + (r & 7) * 16) = make_uint4(0, 0, 0, 0);
    }
    __syncthreads();

    const int p = wave & 1;
    const int gH = p ? 2 : 3, gL = p ? 1 : 0;
    const int a = lane & 15, q = lane >> 4;
    const char* abase = cb + ((a + 1) & 3) * CV_UCOPY + (8 * q + 4 * ((a + 1) >> 2)) * 2;
    const char* bH = kb + (a >= 8 ? CV_KARR : 0) + (8 * gH + (a & 7) + 7) * CV_KROW + 16 * q;
    const char* bL = kb + (a >= 8 ? CV_KARR : 0) + (8 * gL + (a & 7) + 7) * CV_KROW + 16 * q;
    f32x4 accH[4], accL[4];
#pragma unroll
    for (int i = 0; i < 4; ++i) { accH[i] = (f32x4){0.f, 0.f, 0.f, 0.f}; accL[i] = (f32x4){0.f, 0.f, 0.f, 0.f}; }
    auto ldW = [&](int m) -> bf16x8 {
      uint2 x0 = *(const uint2*)(abase + m * 32);
      uint2 x1 = *(const uint2*)(abase + m * 32 + 8);
      uint4 v = make_uint4(x0.x, x0.y, x1.x, x1.y);
      return __builtin_bit_cast(bf16x8, v);
    };
    bf16x8 W0 = ldW(0), W1 = ldW(1);
    const int ndH = 8 * gH + 8, ndL = 8 * gL + 8;
#pragma unroll 1
    for (int dl = 0; dl < ndH; ++dl) {
      bf16x8 W2 = ldW(4 * dl + 2), W3 = ldW(4 * dl + 3), W4 = ldW(4 * dl + 4), W5 = ldW(4 * dl + 5);
      bf16x8 h0 = *(const bf16x8*)(bH - dl * CV_KROW);
      bf16x8 h1 = *(const bf16x8*)(bH - dl * CV_KROW + 64);
      accH[0] = __builtin_amdgcn_mfma_f32_16x16x32_bf16(W0, h0, accH[0], 0, 0, 0);
      accH[1] = __builtin_amdgcn_mfma_f32_16x16x32_bf16(W1, h0, accH[1], 0, 0, 0);
      accH[2] = __builtin_amdgcn_mfma_f32_16x16x32_bf16(W2, h0, accH[2], 0, 0, 0);
      accH[3] = __builtin_amdgcn_mfma_f32_16x16x32_bf16(W3, h0, accH[3], 0, 0, 0);
      accH[0] = __builtin_amdgcn_mfma_f32_16x16x32_bf16(W2, h1, accH[0], 0, 0, 0);
      accH[1] = __builtin_amdgcn_mfma_f32_16x16x32_bf16(W3, h1, accH[1], 0, 0, 0);
      accH[2] = __builtin_amdgcn_mfma_f32_16x16x32_bf16(W4, h1, accH[2], 0, 0, 0);
      accH[3] = __builtin_amdgcn_mfma_f32_16x16x32_bf16(W5, h1, accH[3], 0, 0, 0);
      if (dl < ndL) {
        bf16x8 l0 = *(const bf16x8*)(bL - dl * CV_KROW);
        bf16x8 l1 = *(const bf16x8*)(bL - dl * CV_KROW + 64);
        accL[0] = __builtin_amdgcn_mfma_f32_16x16x32_bf16(W0, l0, accL[0], 0, 0, 0);
        accL[1] = __builtin_amdgcn_mfma_f32_16x16x32_bf16(W1, l0, accL[1], 0, 0, 0);
        accL[2] = __builtin_amdgcn_mfma_f32_16x16x32_bf16(W2, l0, accL[2], 0, 0, 0);
        accL[3] = __builtin_amdgcn_mfma_f32_16x16x32_bf16(W3, l0, accL[3], 0, 0, 0);
        accL[0] = __builtin_amdgcn_mfma_f32_16x16x32_bf16(W2, l1, accL[0], 0, 0, 0);
        accL[1] = __builtin_amdgcn_mfma_f32_16x16x32_bf16(W3, l1, accL[1], 0, 0, 0);
        accL[2] = __builtin_amdgcn_mfma_f32_16x16x32_bf16(W4, l1, accL[2], 0, 0, 0);
        accL[3] = __builtin_amdgcn_mfma_f32_16x16x32_bf16(W5, l1, accL[3], 0, 0, 0);
      }
      W0 = W4; W1 = W5;
    }
    const float dk = bf2f(dskip[d]);
    const u16* xt = (const u16*)(ws + WS_XT) + (size_t)d * L_;
    u16* r1 = (u16*)(ws + WS_R1T) + (size_t)d * L_;
    const int col = (a < 8) ? (8 * gH + a) : (8 * gL + a - 8);
#pragma unroll
    for (int rt = 0; rt < 4; ++rt) {
      int t0 = 64 * col + 16 * rt + 4 * q;
      uint2 uv = *(const uint2*)(ut + t0);
      uint2 xv = *(const uint2*)(xt + t0);
      u32 uw[2] = {uv.x, uv.y}, xw[2] = {xv.x, xv.y};
      float o[4];
#pragma unroll
      for (int r = 0; r < 4; ++r) {
        float sH = accH[rt][r] + __shfl_xor(accH[rt][r], 8, 64);
        float sL = accL[rt][r] + __shfl_xor(accL[rt][r], 8, 64);
        float y = rbf(a < 8 ? sH : sL);
        float un = bf2f((u16)(uw[r >> 1] >> (16 * (r & 1))));
        float xx = bf2f((u16)(xw[r >> 1] >> (16 * (r & 1))));
        float av = rbf(y + rbf(un * dk));
        o[r] = xx + av;
      }
      *(uint2*)(r1 + t0) = make_uint2(pack2(o[0], o[1]), pack2(o[2], o[3]));
    }
  }
}

__global__ __launch_bounds__(256) void k3_kernel(const char* __restrict__ ws, u16* __restrict__ out) {
  __shared__ __attribute__((aligned(16))) f32x4 red[4][16][64];
  const int tid = threadIdx.x;
  const int lane = tid & 63;
  const int wave = __builtin_amdgcn_readfirstlane(tid >> 6);
  const int b = blockIdx.x;
  const int tb = (b & 7) * 4 + ((b >> 3) & 3);
  const int db = b >> 5;
  const u16* ha = (const u16*)(ws + WS_HF) + ((size_t)(tb * 4) * 56 + wave * 14) * 512 + lane * 8;
  const u16* wb = (const u16*)(ws + WS_W2F) + ((size_t)(db * 4) * 56 + wave * 14) * 512 + lane * 8;
  f32x4 acc[4][4];
#pragma unroll
  for (int m = 0; m < 4; ++m)
#pragma unroll
    for (int n = 0; n < 4; ++n) acc[m][n] = (f32x4){0.f, 0.f, 0.f, 0.f};
  constexpr int PF = 4;
  bf16x8 A[PF][4], B[PF][4];
#pragma unroll
  for (int s = 0; s < PF; ++s)
#pragma unroll
    for (int m = 0; m < 4; ++m) {
      A[s][m] = *(const bf16x8*)(ha + ((size_t)m * 56 + s) * 512);
      B[s][m] = *(const bf16x8*)(wb + ((size_t)m * 56 + s) * 512);
    }
#pragma unroll
  for (int s = 0; s < 14; ++s) {
    const int sl = s % PF;
#pragma unroll
    for (int m = 0; m < 4; ++m)
#pragma unroll
      for (int n = 0; n < 4; ++n)
        acc[m][n] = __builtin_amdgcn_mfma_f32_16x16x32_bf16(A[sl][m], B[sl][n], acc[m][n], 0, 0, 0);
    if (s + PF < 14) {
#pragma unroll
      for (int m = 0; m < 4; ++m) {
        A[sl][m] = *(const bf16x8*)(ha + ((size_t)m * 56 + s + PF) * 512);
        B[sl][m] = *(const bf16x8*)(wb + ((size_t)m * 56 + s + PF) * 512);
      }
    }
  }
#pragma unroll
  for (int m = 0; m < 4; ++m)
#pragma unroll
    for (int n = 0; n < 4; ++n) red[wave][m * 4 + n][lane] = acc[m][n];
  __syncthreads();
  const int c = lane & 15, Q = lane >> 4;
  const u16* r1 = (const u16*)(ws + WS_R1T);
#pragma unroll
  for (int n = 0; n < 4; ++n) {
    f32x4 v = red[0][wave * 4 + n][lane];
#pragma unroll
    for (int w = 1; w < 4; ++w) {
      f32x4 t = red[w][wave * 4 + n][lane];
      v[0] += t[0]; v[1] += t[1]; v[2] += t[2]; v[3] += t[3];
    }
    int dcol = db * 64 + n * 16 + c;
    int tok0 = tb * 64 + wave * 16 + 4 * Q;
    uint2 rv = *(const uint2*)(r1 + (size_t)dcol * L_ + tok0);
    u32 rw[2] = {rv.x, rv.y};
#pragma unroll
    for (int r = 0; r < 4; ++r) {
      float rr = bf2f((u16)(rw[r >> 1] >> (16 * (r & 1))));
      out[(size_t)(tok0 + r) * D_ + dcol] = f2bf(rr + rbf(v[r]));
    }
  }
}

extern "C" void kernel_launch(void* const* d_in, const int* in_sizes, int n_in, void* d_out, int out_size,
                              void* d_ws, size_t ws_size, hipStream_t stream) {
  const u16* x = (const u16*)d_in[0];
  const u16* logA = (const u16*)d_in[1];
  const u16* Aim = (const u16*)d_in[2];
  const u16* Cre = (const u16*)d_in[3];
  const u16* Cim = (const u16*)d_in[4];
  const u16* logdt = (const u16*)d_in[5];
  const u16* dskip = (const u16*)d_in[6];
  const u16* wg = (const u16*)d_in[8];
  const u16* wu = (const u16*)d_in[9];
  const u16* wd = (const u16*)d_in[10];
  char* ws = (char*)d_ws;
  k1_kernel<<<K1_GRID, 256, 0, stream>>>(x, logA, Aim, Cre, Cim, logdt, wg, wu, wd, ws);
  k2_kernel<<<512, 256, 0, stream>>>(dskip, ws);
  k3_kernel<<<256, 256, 0, stream>>>(ws, (u16*)d_out);
}
```

```cpp
#include <hip/hip_runtime.h>
#include <hip/hip_bf16.h>

typedef __attribute__((ext_vector_type(8))) short bf16x8;
typedef __attribute__((ext_vector_type(4))) float f32x4;
typedef unsigned short u16;
typedef unsigned int u32;
typedef __attribute__((ext_vector_type(4))) unsigned int u32x4;

constexpr int L_ = 2048;
constexpr int D_ = 512;
constexpr int N_ = 128;
constexpr int F_ = 1792;

constexpr size_t WS_XNF = 0;
constexpr size_t WS_XNT = WS_XNF + (size_t)L_ * D_ * 2;
constexpr size_t WS_XT = WS_XNT + (size_t)L_ * D_ * 2;
constexpr size_t WS_W1F = WS_XT + (size_t)L_ * D_ * 2;
constexpr size_t WS_W2F = WS_W1F + (size_t)D_ * F_ * 2 * 2;
constexpr size_t WS_KC = WS_W2F + (size_t)D_ * F_ * 2;
constexpr size_t WS_HF = WS_KC + (size_t)D_ * 2 * L_ * 2;
constexpr size_t WS_R1T = WS_HF + (size_t)L_ * F_ * 2;
constexpr size_t WS_END = WS_R1T + (size_t)D_ * L_ * 2;

#define STAMP(kid, idx)
__device__ __forceinline__ float bf2f(u16 v) { return __uint_as_float(((u32)v) << 16); }
__device__ __forceinline__ u16 f2bf(float f) {
  __bf16 b = (__bf16)f;
  return __builtin_bit_cast(u16, b);
}
__device__ __forceinline__ float rbf(float f) { return bf2f(f2bf(f)); }
__device__ __forceinline__ u32 pack2(float lo, float hi) { return (u32)f2bf(lo) | ((u32)f2bf(hi) << 16); }

__device__ __forceinline__ float wave_sum(float v) {
  int x = __float_as_int(v);
  v += __int_as_float(__builtin_amdgcn_update_dpp(0, x, 0xB1, 0xf, 0xf, true));
  x = __float_as_int(v);
  v += __int_as_float(__builtin_amdgcn_update_dpp(0, x, 0x4E, 0xf, 0xf, true));
  x = __float_as_int(v);
  v += __int_as_float(__builtin_amdgcn_update_dpp(0, x, 0x141, 0xf, 0xf, true));
  x = __float_as_int(v);
  v += __int_as_float(__builtin_amdgcn_update_dpp(0, x, 0x140, 0xf, 0xf, true));
  x = __float_as_int(v);
  float a = __int_as_float(__builtin_amdgcn_readlane(x, 0));
  float b = __int_as_float(__builtin_amdgcn_readlane(x, 16));
  float c = __int_as_float(__builtin_amdgcn_readlane(x, 32));
  float d = __int_as_float(__builtin_amdgcn_readlane(x, 48));
  return (a + b) + (c + d);
}

constexpr int K1_NB_KGEN = 512;
constexpr int K1_NB_LN = 128;
constexpr int K1_NB_W1 = 224;
constexpr int K1_NB_W2 = 112;
constexpr int K1_GRID = K1_NB_KGEN + K1_NB_LN + K1_NB_W1 + K1_NB_W2;

__global__ __launch_bounds__(256) void k1_kernel(
    const u16* __restrict__ x, const u16* __restrict__ logA, const u16* __restrict__ Aim,
    const u16* __restrict__ Cre, const u16* __restrict__ Cim, const u16* __restrict__ logdt,
    const u16* __restrict__ wg, const u16* __restrict__ wu, const u16* __restrict__ wd,
    char* __restrict__ ws) {
  __shared__ __attribute__((aligned(16))) char smem[2 * 16 * 520 * 2];
  const int tid = threadIdx.x;
  const int lane = tid & 63;
  const int wave = tid >> 6;
  int b = blockIdx.x;
  STAMP(0, 0);

  if (b < K1_NB_KGEN) {
    const int d = b;
    f32x4* prm = (f32x4*)smem;
    float* tab = (float*)(smem + 4096);
    if (tid < N_) {
      float dt = rbf(expf(bf2f(logdt[d])));
      float ar = rbf(expf(bf2f(logA[d * N_ + tid])));
      float zr = -ar * dt;
      float zi = bf2f(Aim[d * N_ + tid]) * dt;
      const float I_HI = 0.15915494309189535f;
      const float I_LO = 6.4206382e-9f;
      float zh = zi * I_HI;
      float zl = __builtin_fmaf(zi, I_LO, __builtin_fmaf(zi, I_HI, -zh));
      prm[2 * tid] = (f32x4){zr * 1.4426950408889634f, zh, zl, 0.f};
      float cr = bf2f(Cre[d * N_ + tid]) * dt, ci = bf2f(Cim[d * N_ + tid]) * dt;
      prm[2 * tid + 1] = (f32x4){cr, ci, -cr, -ci};
    }
    __syncthreads();
    STAMP(0, 1);
    {
      const int i = lane & 15, kk = lane >> 4;
      const float pa0 = (float)((i & 7) + 128 * (i >> 3));
      const float pa1 = (float)(i < 8 ? 256 + 128 * (i >> 2) + 2 * (i & 3) : 512 + 128 * ((i - 8) >> 1) + 4 * (i & 1));
      const float pa2 = (float)(1024 + 128 * (i > 8 ? 8 : i));
      const float pb0 = (float)(8 * i);
      f32x4 acc[3];
#pragma unroll
      for (int sc = 0; sc < 3; ++sc) acc[sc] = (f32x4){0.f, 0.f, 0.f, 0.f};
      auto cexp = [&](const f32x4& z, float pp, float& er, float& ei) {
        float e = __builtin_amdgcn_exp2f(z[0] * pp);
        float r = __builtin_rintf(z[1] * pp);
        float f = __builtin_fmaf(z[1], pp, -r);
        f = __builtin_fmaf(z[2], pp, f);
        er = e * __builtin_amdgcn_cosf(f);
        asm("" : "+v"(er));
        ei = e * __builtin_amdgcn_sinf(f);
        asm("" : "+v"(ei));
      };
      auto split = [&](float re, float im, u32& hi, u32& lo) {
        hi = pack2(re, im);
        float rl = re - __uint_as_float(hi << 16);
        float il = im - __uint_as_float(hi & 0xffff0000u);
        lo = pack2(rl, il);
      };
#pragma unroll
      for (int g = 0; g < 2; ++g) {
        u32x4 ah[3], al[3], bh, bl;
#pragma unroll
        for (int m = 0; m < 4; ++m) {
          const int n = 16 * (wave * 2 + g) + 4 * kk + m;
          f32x4 z = prm[2 * n], cc = prm[2 * n + 1];
          float er, ei;
          u32 h, l;
          cexp(z, pa0, er, ei); split(er, ei, h, l); ah[0][m] = h; al[0][m] = l;
          cexp(z, pa1, er, ei); split(er, ei, h, l); ah[1][m] = h; al[1][m] = l;
          cexp(z, pa2, er, ei); split(er, ei, h, l); ah[2][m] = h; al[2][m] = l;
          cexp(z, pb0, er, ei);
          float t0 = cc[1] * ei;
          asm("" : "+v"(t0));
          float bre = __builtin_fmaf(cc[0], er, -t0);
          float t1 = cc[2] * ei;
          asm("" : "+v"(t1));
          float bim = __builtin_fmaf(cc[3], er, t1);
          split(bre, bim, h, l); bh[m] = h; bl[m] = l;
        }
#pragma unroll
        for (int sc = 0; sc < 3; ++sc) {
          acc[sc] = __builtin_amdgcn_mfma_f32_16x16x32_bf16(__builtin_bit_cast(bf16x8, al[sc]), __builtin_bit_cast(bf16x8, bh), acc[sc], 0, 0, 0);
          acc[sc] = __builtin_amdgcn_mfma_f32_16x16x32_bf16(__builtin_bit_cast(bf16x8, ah[sc]), __builtin_bit_cast(bf16x8, bl), acc[sc], 0, 0, 0);
          acc[sc] = __builtin_amdgcn_mfma_f32_16x16x32_bf16(__builtin_bit_cast(bf16x8, ah[sc]), __builtin_bit_cast(bf16x8, bh), acc[sc], 0, 0, 0);
        }
      }
      float* tw = tab + wave * 768;
#pragma unroll
      for (int r = 0; r < 4; ++r) {
        int row = 4 * kk + r;
#pragma unroll
        for (int sc = 0; sc < 3; ++sc) tw[sc * 256 + 16 * row + i] = acc[sc][r];
      }
    }
    __syncthreads();
    STAMP(0, 2);
    {
      u16* kc = (u16*)(ws + WS_KC) + (size_t)d * 2 * L_;
      u32 hi[4], lo[4];
#pragma unroll
      for (int e = 0; e < 8; ++e) {
        int o = tid * 8 + e;
        int l = (o & ~63) + 63 - (o & 63);
        int p = (int)rbf((float)l);
        int idx;
        {
          int j = (p & 127) >> 3;
          int row;
          if (p < 256) row = (p & 7) + 8 * (p >> 7);
          else if (p < 512) row = 16 + (((p - 256) >> 7) << 2) + ((p & 7) >> 1);
          else if (p < 1024) row = 24 + (((p - 512) >> 7) << 1) + ((p & 7) >> 2);
          else row = 32 + ((p - 1024) >> 7);
          idx = row * 16 + j;
        }
        float k = 2.0f * ((tab[idx] + tab[768 + idx]) + (tab[2 * 768 + idx] + tab[3 * 768 + idx]));
        u16 h = f2bf(k);
        u16 lw = f2bf(k - bf2f(h));
        if (e & 1) { hi[e >> 1] |= ((u32)h) << 16; lo[e >> 1] |= ((u32)lw) << 16; }
        else { hi[e >> 1] = h; lo[e >> 1] = lw; }
      }
      *(uint4*)(kc + tid * 8) = make_uint4(hi[0], hi[1], hi[2], hi[3]);
      *(uint4*)(kc + L_ + tid * 8) = make_uint4(lo[0], lo[1], lo[2], lo[3]);
    }
    STAMP(0, 3);
    return;
  }
  b -= K1_NB_KGEN;
  __builtin_amdgcn_s_setprio(3);
  if (b < K1_NB_LN) {
    constexpr int LDW = 520;
    u16* sx = (u16*)smem;
    u16* sn = (u16*)(smem + 16 * LDW * 2);
    const int row0 = b * 16;
    u32x4 v[4];
#pragma unroll
    for (int r = 0; r < 4; ++r) v[r] = *(const u32x4*)(x + (size_t)(row0 + wave * 4 + r) * D_ + lane * 8);
#pragma unroll
    for (int r = 0; r < 4; ++r) {
      float f[8];
#pragma unroll
      for (int e = 0; e < 4; ++e) { f[2 * e] = bf2f((u16)(v[r][e] & 0xffff)); f[2 * e + 1] = bf2f((u16)(v[r][e] >> 16)); }
      float s = 0.f;
#pragma unroll
      for (int e = 0; e < 8; ++e) s += f[e];
      s = wave_sum(s);
      float mean = s * (1.0f / D_);
      float q = 0.f;
#pragma unroll
      for (int e = 0; e < 8; ++e) { float dd = f[e] - mean; q += dd * dd; }
      q = wave_sum(q);
      float var = q * (1.0f / D_);
      float mb = rbf(mean);
      float vb = rbf(var);
      float den = rbf(sqrtf(rbf(vb + 1e-5f)));
      u32x4 ow;
#pragma unroll
      for (int e = 0; e < 4; ++e) {
        float a0 = rbf(f[2 * e] - mb) / den;
        float a1 = rbf(f[2 * e + 1] - mb) / den;
        ow[e] = pack2(a0, a1);
      }
      int lr = wave * 4 + r;
      *(u32x4*)(sx + lr * LDW + lane * 8) = v[r];
      *(u32x4*)(sn + lr * LDW + lane * 8) = ow;
    }
    STAMP(0, 1);
    __syncthreads();
    STAMP(0, 2);
    u16* xnf = (u16*)(ws + WS_XNF) + (size_t)b * 16 * 512;
#pragma unroll
    for (int it = 0; it < 4; ++it) {
      int pc = it * 256 + tid;
      int ln = pc & 63, ks = pc >> 6;
      int c = ln & 15, q = ln >> 4;
      u32x4 val = *(const u32x4*)(sn + c * LDW + ks * 32 + q * 8);
      *(u32x4*)(xnf + (size_t)pc * 8) = val;
    }
    u16* xnt = (u16*)(ws + WS_XNT);
    u16* xt = (u16*)(ws + WS_XT);
#pragma unroll
    for (int arr = 0; arr < 2; ++arr) {
      const u16* sp = arr ? sx : sn;
      u16* dp = arr ? xt : xnt;
      u32 w[16];
#pragma unroll
      for (int r = 0; r < 16; ++r) w[r] = *(const u32*)(sp + r * LDW + 2 * tid);
      u32x4 lo0, lo1, hi0, hi1;
#pragma unroll
      for (int e = 0; e < 4; ++e) {
        lo0[e] = __builtin_amdgcn_perm(w[2 * e + 1], w[2 * e], 0x05040100);
        hi0[e] = __builtin_amdgcn_perm(w[2 * e + 1], w[2 * e], 0x07060302);
        lo1[e] = __builtin_amdgcn_perm(w[8 + 2 * e + 1], w[8 + 2 * e], 0x05040100);
        hi1[e] = __builtin_amdgcn_perm(w[8 + 2 * e + 1], w[8 + 2 * e], 0x07060302);
      }
      u16* d0 = dp + (size_t)(2 * tid) * L_ + row0;
      *(u32x4*)(d0) = lo0;
      *(u32x4*)(d0 + 8) = lo1;
      *(u32x4*)(d0 + L_) = hi0;
      *(u32x4*)(d0 + L_ + 8) = hi1;
    }
    STAMP(0, 3);
    return;
  }
  b -= K1_NB_LN;
  {
    constexpr int LDW = 136;
    u16* st = (u16*)smem;
    const u16* src;
    int ld, ks0, nch;
    int kind;
    if (b < K1_NB_W1) {
      kind = b / 112;
      int r = b % 112;
      ks0 = (r / 14) * 2; nch = r % 14;
      src = kind == 0 ? wg : wu;
      ld = F_;
    } else {
      int r = b - K1_NB_W1;
      kind = 2;
      ks0 = (r >> 2) * 2; nch = r & 3;
      src = wd;
      ld = D_;
    }
    u32x4 val[4];
#pragma unroll
    for (int h = 0; h < 4; ++h) {
      int r = (tid >> 4) + 16 * h, cc = (tid & 15) * 8;
      val[h] = *(const u32x4*)(src + (size_t)(ks0 * 32 + r) * ld + nch * 128 + cc);
    }
#pragma unroll
    for (int h = 0; h < 4; ++h) {
      int r = (tid >> 4) + 16 * h, cc = (tid & 15) * 8;
      *(u32x4*)(st + r * LDW + cc) = val[h];
    }
    __syncthreads();
#pragma unroll
    for (int h = 0; h < 4; ++h) {
      int pc = (h & 1) * 256 + tid;
      int ks = ks0 + (h >> 1);
      int ln = pc & 63, nt = pc >> 6;
      int a = ln & 15, q = ln >> 4;
      const u16* sr = st + ((h >> 1) * 32 + 8 * q) * LDW + nt * 16 + a;
      u32x4 w;
#pragma unroll
      for (int e = 0; e < 4; ++e) w[e] = (u32)sr[(2 * e) * LDW] | ((u32)sr[(2 * e + 1) * LDW] << 16);
      int ft = nch * 8 + nt;
      u16* dst;
      if (kind < 2) {
        int fg = ft / 7, tau = ft % 7 + 7 * kind;
        dst = (u16*)(ws + WS_W1F) + ((size_t)(fg * 16 + ks) * 14 + tau) * 512 + ln * 8;
      } else {
        dst = (u16*)(ws + WS_W2F) + ((size_t)ft * 56 + ks) * 512 + ln * 8;
      }
      *(u32x4*)dst = w;
    }
  }
}

constexpr int CV_UCOPY = 4416;
constexpr int CV_KROW = 160;
constexpr int CV_KARR = 6400;
constexpr int CV_CH = 4 * CV_UCOPY + 2 * CV_KARR;
constexpr int G1_STAGE = 22 * 1024;
constexpr int K2_LDS = 3 * G1_STAGE;

typedef __attribute__((ext_vector_type(2))) unsigned int u32x2;
#define MF(acc, A, B) acc = __builtin_amdgcn_mfma_f32_16x16x32_bf16(A, B, acc, 0, 0, 0)
template <int OFF> __device__ __forceinline__ u32x2 dsr64(u32 addr) {
  u32x2 r;
  asm volatile("ds_read_b64 %0, %1 offset:%2" : "=v"(r) : "v"(addr), "i"(OFF));
  return r;
}
template <int OFF> __device__ __forceinline__ bf16x8 dsr128(u32 addr) {
  bf16x8 r;
  asm volatile("ds_read_b128 %0, %1 offset:%2" : "=v"(r) : "v"(addr), "i"(OFF));
  return r;
}
template <int M> __device__ __forceinline__ bf16x8 ldW(u32 aaddr) {
  u32x2 x0 = dsr64<M * 32>(aaddr);
  u32x2 x1 = dsr64<M * 32 + 8>(aaddr);
  u32x4 v = {x0[0], x0[1], x1[0], x1[1]};
  return __builtin_bit_cast(bf16x8, v);
}
template <int N> __device__ __forceinline__ void wait_lgkm() {
  asm volatile("s_waitcnt lgkmcnt(%0)" ::"i"(N) : "memory");
  __builtin_amdgcn_sched_barrier(0);
}
struct ConvStep { bf16x8 w2, w3, w4, w5, h0, h1, l0, l1; };
template <int DL, int NDL> __device__ __forceinline__ void conv_load(ConvStep& st, u32 aaddr, u32 haddr, u32 laddr) {
  st.w2 = ldW<4 * DL + 2>(aaddr); st.w3 = ldW<4 * DL + 3>(aaddr);
  st.w4 = ldW<4 * DL + 4>(aaddr); st.w5 = ldW<4 * DL + 5>(aaddr);
  st.h0 = dsr128<(31 - DL) * CV_KROW>(haddr); st.h1 = dsr128<(31 - DL) * CV_KROW + 64>(haddr);
  if constexpr (DL < NDL) {
    st.l0 = dsr128<(31 - DL) * CV_KROW>(laddr); st.l1 = dsr128<(31 - DL) * CV_KROW + 64>(laddr);
  }
}
#define SB __builtin_amdgcn_sched_barrier(0)
template <int DL, int NDH, int NDL>
__device__ __forceinline__ void conv_step(ConvStep (&st)[3], bf16x8& w0, bf16x8& w1, u32 aaddr, u32 haddr, u32 laddr,
                                          f32x4 (&accH)[4], f32x4 (&accL)[4]) {
  if constexpr (DL < NDH) {
    constexpr int CN1 = (DL + 1 < NDH) ? ((DL + 1 < NDL) ? 12 : 10) : 0;
    constexpr bool PF = (DL + 2 < NDH);
    constexpr int DN = DL + 2;
    wait_lgkm<CN1>();
    ConvStep& c = st[DL % 3];
    ConvStep& n = st[(DL + 2) % 3];
    if constexpr (DL < NDL) {
      MF(accH[0], w0, c.h0); SB; if constexpr (PF) n.w2 = ldW<4 * DN + 2>(aaddr); SB;
      MF(accH[1], w1, c.h0); MF(accH[2], c.w2, c.h0); SB; if constexpr (PF) n.w3 = ldW<4 * DN + 3>(aaddr); SB;
      MF(accH[3], c.w3, c.h0); MF(accL[0], w0, c.l0); SB; if constexpr (PF) n.w4 = ldW<4 * DN + 4>(aaddr); SB;
      MF(accL[1], w1, c.l0); MF(accL[2], c.w2, c.l0); SB; if constexpr (PF) n.w5 = ldW<4 * DN + 5>(aaddr); SB;
      MF(accL[3], c.w3, c.l0); MF(accH[0], c.w2, c.h1); SB; if constexpr (PF) n.h0 = dsr128<(31 - DN) * CV_KROW>(haddr); SB;
      MF(accH[1], c.w3, c.h1); MF(accH[2], c.w4, c.h1); SB; if constexpr (PF) n.h1 = dsr128<(31 - DN) * CV_KROW + 64>(haddr); SB;
      MF(accH[3], c.w5, c.h1); MF(accL[0], c.w2, c.l1); SB; if constexpr (PF && DN < NDL) n.l0 = dsr128<(31 - DN) * CV_KROW>(laddr); SB;
      MF(accL[1], c.w3, c.l1); MF(accL[2], c.w4, c.l1); SB; if constexpr (PF && DN < NDL) n.l1 = dsr128<(31 - DN) * CV_KROW + 64>(laddr); SB;
      MF(accL[3], c.w5, c.l1); SB;
    } else {
      MF(accH[0], w0, c.h0); SB; if constexpr (PF) n.w2 = ldW<4 * DN + 2>(aaddr); SB;
      MF(accH[1], w1, c.h0); SB; if constexpr (PF) n.w3 = ldW<4 * DN + 3>(aaddr); SB;
      MF(accH[2], c.w2, c.h0); SB; if constexpr (PF) n.w4 = ldW<4 * DN + 4>(aaddr); SB;
      MF(accH[3], c.w3, c.h0); SB; if constexpr (PF) n.w5 = ldW<4 * DN + 5>(aaddr); SB;
      MF(accH[0], c.w2, c.h1); SB; if constexpr (PF) n.h0 = dsr128<(31 - DN) * CV_KROW>(haddr); SB;
      MF(accH[1], c.w3, c.h1); SB; if constexpr (PF) n.h1 = dsr128<(31 - DN) * CV_KROW + 64>(haddr); SB;
      MF(accH[2], c.w4, c.h1); MF(accH[3], c.w5, c.h1); SB;
    }
    w0 = c.w4; w1 = c.w5;
    conv_step<DL + 1, NDH, NDL>(st, w0, w1, aaddr, haddr, laddr, accH, accL);
  }
}
template <int NDH, int NDL>
__device__ __forceinline__ void conv_main(u32 aaddr, u32 haddr, u32 laddr, f32x4 (&accH)[4], f32x4 (&accL)[4]) {
  ConvStep st[3];
  bf16x8 w0 = ldW<0>(aaddr), w1 = ldW<1>(aaddr);
  conv_load<0, NDL>(st[0], aaddr, haddr, laddr);
  conv_load<1, NDL>(st[1], aaddr, haddr, laddr);
  conv_step<0, NDH, NDL>(st, w0, w1, aaddr, haddr, laddr, accH, accL);
}

__device__ __forceinline__ float gelu_tanh(float g) {
  const float c0 = 2.0f * 0.7978845608028654f * 1.4426950408889634f;
  const float c1 = c0 * 0.044715f;
  float g2 = g * g;
  float z = g * __builtin_fmaf(g2, c1, c0);
  float e = __builtin_amdgcn_exp2f(z);
  float r = __builtin_amdgcn_rcpf(1.0f + e);
  return __builtin_fmaf(-g, r, g);
}

__global__ __launch_bounds__(256, 2) void k2_kernel(const u16* __restrict__ dskip, char* __restrict__ ws) {
  __shared__ __attribute__((aligned(16))) char smem[K2_LDS];
  const int tid = threadIdx.x;
  const int lane = tid & 63;
  const int wave = __builtin_amdgcn_readfirstlane(tid >> 6);
  const int b = blockIdx.x;
  STAMP(1, 0);

  if (b < 256) {
    const int tt = ((b & 7) >> 2) * 8 + (b >> 5);
    const int fg = (b & 3) * 4 + ((b >> 3) & 3);
    const u16* w1 = (const u16*)(ws + WS_W1F) + (size_t)fg * 16 * 14 * 512;
    const u16* xf = (const u16*)(ws + WS_XNF) + (size_t)(tt * 8) * 16 * 512;
    f32x4 acc[14][2];
#pragma unroll
    for (int i = 0; i < 14; ++i) { acc[i][0] = (f32x4){0.f, 0.f, 0.f, 0.f}; acc[i][1] = (f32x4){0.f, 0.f, 0.f, 0.f}; }
    const u16* gsrc[6];
    int gdst[6];
    int gstep[6];
#pragma unroll
    for (int i = 0; i < 6; ++i) {
      int p = wave + 4 * i;
      if (p >= 22) p -= 22;
      if (p < 14) { gsrc[i] = w1 + p * 512 + lane * 8; gstep[i] = 14 * 512; }
      else { gsrc[i] = xf + (p - 14) * 16 * 512 + lane * 8; gstep[i] = 512; }
      gdst[i] = p * 1024 + lane * 16;
    }
    const int rot = 0;
    auto issue1 = [&](int ks, int i) {
      char* sb = smem + (ks % 3) * G1_STAGE;
      __builtin_amdgcn_global_load_lds((const __attribute__((address_space(1))) u32*)(gsrc[i] + ((ks + rot) & 15) * gstep[i]),
                                       (__attribute__((address_space(3))) u32*)(sb + gdst[i]), 16, 0, 0);
    };
#pragma unroll
    for (int i = 0; i < 6; ++i) issue1(0, i);
#pragma unroll
    for (int i = 0; i < 6; ++i) issue1(1, i);
#pragma unroll
    for (int i = 0; i < 6; ++i) issue1(2, i);
    asm volatile("s_waitcnt vmcnt(12)" ::: "memory");
    __builtin_amdgcn_s_barrier();
    bf16x8 alo[7], ahi[7], t0, t1;
    {
      const char* buf = smem + lane * 16;
      t0 = *(const bf16x8*)(buf + (14 + 2 * wave) * 1024);
      t1 = *(const bf16x8*)(buf + (15 + 2 * wave) * 1024);
#pragma unroll
      for (int t = 0; t < 7; ++t) alo[t] = *(const bf16x8*)(buf + t * 1024);
    }
#pragma unroll
    for (int ks = 0; ks < 16; ++ks) {
      const char* buf = smem + (ks % 3) * G1_STAGE + lane * 16;
#pragma unroll
      for (int t = 0; t < 7; ++t) {
        acc[t][0] = __builtin_amdgcn_mfma_f32_16x16x32_bf16(alo[t], t0, acc[t][0], 0, 0, 0);
        acc[t][1] = __builtin_amdgcn_mfma_f32_16x16x32_bf16(alo[t], t1, acc[t][1], 0, 0, 0);
        __builtin_amdgcn_sched_barrier(0);
        ahi[t] = *(const bf16x8*)(buf + (7 + t) * 1024);
        __builtin_amdgcn_sched_barrier(0);
      }
      bf16x8 n0 = t0, n1 = t1;
      if (ks < 15) {
        if (ks < 14) asm volatile("s_waitcnt vmcnt(6) lgkmcnt(0)" ::: "memory");
        else asm volatile("s_waitcnt vmcnt(0) lgkmcnt(0)" ::: "memory");
        __builtin_amdgcn_s_barrier();
        __builtin_amdgcn_sched_barrier(0);
      }
      const char* nb = smem + ((ks + 1) % 3) * G1_STAGE + lane * 16;
#pragma unroll
      for (int t = 0; t < 7; ++t) {
        acc[7 + t][0] = __builtin_amdgcn_mfma_f32_16x16x32_bf16(ahi[t], t0, acc[7 + t][0], 0, 0, 0);
        __builtin_amdgcn_sched_barrier(0);
        if (ks < 15) {
          alo[t] = *(const bf16x8*)(nb + t * 1024);
          if (t == 0) n0 = *(const bf16x8*)(nb + (14 + 2 * wave) * 1024);
          if (t == 1) n1 = *(const bf16x8*)(nb + (15 + 2 * wave) * 1024);
          __builtin_amdgcn_sched_barrier(0);
        }
        acc[7 + t][1] = __builtin_amdgcn_mfma_f32_16x16x32_bf16(ahi[t], t1, acc[7 + t][1], 0, 0, 0);
        if (t < 6 && ks + 3 < 16) {
          __builtin_amdgcn_sched_barrier(0);
          issue1(ks + 3, t);
        }
        __builtin_amdgcn_sched_barrier(0);
      }
      t0 = n0; t1 = n1;
    }
    STAMP(1, 2);
    u16* hf = (u16*)(ws + WS_HF);
    const int c = lane & 15, Q = lane >> 4;
#pragma unroll
    for (int t = 0; t < 7; ++t) {
      int ft = fg * 7 + t;
      int ks2 = ft >> 1;
      int q = 2 * (ft & 1) + (Q >> 1);
#pragma unroll
      for (int m = 0; m < 2; ++m) {
        float hv[4];
#pragma unroll
        for (int r = 0; r < 4; ++r) {
          float g = rbf(acc[t][m][r]);
          float u = rbf(acc[t + 7][m][r]);
          hv[r] = rbf(gelu_tanh(g)) * u;
        }
        int tile = tt * 8 + 2 * wave + m;
        uint2 o = make_uint2(pack2(hv[0], hv[1]), pack2(hv[2], hv[3]));
        *(uint2*)(hf + (((size_t)tile * 56 + ks2) * 64 + (c + 16 * q)) * 8 + 4 * (Q & 1)) = o;
      }
    }
    STAMP(1, 3);
    return;
  }

  {
    const int chl = wave >> 1;
    const int d = (b - 256) * 2 + chl;
    const int t128 = tid & 127;
    char* cb = smem + chl * CV_CH;
    const u16* ut = (const u16*)(ws + WS_XNT) + (size_t)d * L_;
    const u16* kc = (const u16*)(ws + WS_KC) + (size_t)d * 2 * L_;
    char* kb = cb + 4 * CV_UCOPY;
    {
      u32x4 v0[3], v1[3], kv[4];
#pragma unroll
      for (int it = 0; it < 3; ++it) {
        int cch = it * 128 + t128 - 8;
        int c0 = cch < 0 ? 0 : (cch > 255 ? 255 : cch);
        int c1 = cch + 1 < 0 ? 0 : (cch + 1 > 255 ? 255 : cch + 1);
        v0[it] = *(const u32x4*)(ut + c0 * 8);
        v1[it] = *(const u32x4*)(ut + c1 * 8);
      }
#pragma unroll
      for (int it = 0; it < 4; ++it) {
        int pc = it * 128 + t128;
        kv[it] = *(const u32x4*)(kc + pc * 8);
      }
      __builtin_amdgcn_sched_barrier(0);
#pragma unroll
      for (int it = 0; it < 3; ++it) {
        int X = it * 128 + t128;
        int cch = X - 8;
        bool ok0 = (cch >= 0 && cch < 256), ok1 = (cch + 1 >= 0 && cch + 1 < 256);
        u32 w[6] = {ok0 ? v0[it][0] : 0u, ok0 ? v0[it][1] : 0u, ok0 ? v0[it][2] : 0u, ok0 ? v0[it][3] : 0u,
                    ok1 ? v1[it][0] : 0u, ok1 ? v1[it][1] : 0u};
        u32 s1[5];
#pragma unroll
        for (int e = 0; e < 5; ++e) s1[e] = __builtin_amdgcn_alignbit(w[e + 1], w[e], 16);
        if (X < 276) {
          *(u32x4*)(cb + 0 * CV_UCOPY + X * 16) = (u32x4){w[0], w[1], w[2], w[3]};
          *(u32x4*)(cb + 1 * CV_UCOPY + X * 16) = (u32x4){s1[0], s1[1], s1[2], s1[3]};
          *(u32x4*)(cb + 2 * CV_UCOPY + X * 16) = (u32x4){w[1], w[2], w[3], w[4]};
          *(u32x4*)(cb + 3 * CV_UCOPY + X * 16) = (u32x4){s1[1], s1[2], s1[3], s1[4]};
        }
      }
#pragma unroll
      for (int it = 0; it < 4; ++it) {
        int pc = it * 128 + t128;
        int arr = pc >> 8, row = (pc >> 3) & 31, ch = pc & 7;
        *(u32x4*)(kb + arr * CV_KARR + (row + 7) * CV_KROW + ch * 16) = kv[it];
      }
    }
    if (t128 < 112) {
      int arr = t128 / 56, r = t128 % 56;
      *(uint4*)(kb + arr * CV_KARR + (r >> 3) * CV_KROW + (r & 7) * 16) = make_uint4(0, 0, 0, 0);
    }
    __syncthreads();
    STAMP(1, 1);

    const int p = wave & 1;
    const int gH = p ? 2 : 3, gL = p ? 1 : 0;
    const int a = lane & 15, q = lane >> 4;
    const char* abase = cb + ((a + 1) & 3) * CV_UCOPY + (8 * q + 4 * ((a + 1) >> 2)) * 2;
    const char* bH = kb + (a >= 8 ? CV_KARR : 0) + (8 * gH + (a & 7) + 7) * CV_KROW + 16 * q;
    const char* bL = kb + (a >= 8 ? CV_KARR : 0) + (8 * gL + (a & 7) + 7) * CV_KROW + 16 * q;
    f32x4 accH[4], accL[4];
#pragma unroll
    for (int i = 0; i < 4; ++i) { accH[i] = (f32x4){0.f, 0.f, 0.f, 0.f}; accL[i] = (f32x4){0.f, 0.f, 0.f, 0.f}; }
    const u16* xt = (const u16*)(ws + WS_XT) + (size_t)d * L_;
    const int col = (a < 8) ? (8 * gH + a) : (8 * gL + a - 8);
    u32x2 uvp[4], xvp[4];
#pragma unroll
    for (int rt = 0; rt < 4; ++rt) {
      int t0 = 64 * col + 16 * rt + 4 * q;
      uvp[rt] = *(const u32x2*)(ut + t0);
      xvp[rt] = *(const u32x2*)(xt + t0);
    }
    const float dk = bf2f(dskip[d]);
    __builtin_amdgcn_sched_barrier(0);
    {
      typedef __attribute__((address_space(3))) const char* lptr;
      u32 aaddr = (u32)(size_t)(lptr)abase;
      u32 haddr = (u32)(size_t)(lptr)bH - 31 * CV_KROW;
      u32 laddr = (u32)(size_t)(lptr)bL - 31 * CV_KROW;
      if (p) conv_main<24, 16>(aaddr, haddr, laddr, accH, accL);
      else conv_main<32, 8>(aaddr, haddr, laddr, accH, accL);
    }
    STAMP(1, 2);
    u16* r1 = (u16*)(ws + WS_R1T) + (size_t)d * L_;
#pragma unroll
    for (int rt = 0; rt < 4; ++rt) {
      int t0 = 64 * col + 16 * rt + 4 * q;
      float o[4];
#pragma unroll
      for (int r = 0; r < 4; ++r) {
        float mine = (a < 8) ? accH[rt][r] : accL[rt][r];
        float give = (a < 8) ? accL[rt][r] : accH[rt][r];
        float other = __int_as_float(__builtin_amdgcn_update_dpp(0, __float_as_int(give), 0x128, 0xf, 0xf, true));
        float y = rbf(mine + other);
        float un = bf2f((u16)(uvp[rt][r >> 1] >> (16 * (r & 1))));
        float xx = bf2f((u16)(xvp[rt][r >> 1] >> (16 * (r & 1))));
        float av = rbf(y + rbf(un * dk));
        o[r] = xx + av;
      }
      *(uint2*)(r1 + t0) = make_uint2(pack2(o[0], o[1]), pack2(o[2], o[3]));
    }
    STAMP(1, 3);
  }
}

__global__ __launch_bounds__(256) void k3_kernel(char* __restrict__ ws, u16* __restrict__ out) {
  __shared__ __attribute__((aligned(16))) f32x4 red[4][16][64];
  const int tid = threadIdx.x;
  const int lane = tid & 63;
  const int wave = __builtin_amdgcn_readfirstlane(tid >> 6);
  const int b = blockIdx.x;
  STAMP(2, 0);
  const int tb = (b & 7) * 4 + ((b >> 3) & 3);
  const int db = b >> 5;
  const u16* ha = (const u16*)(ws + WS_HF) + ((size_t)(tb * 4) * 56 + wave * 14) * 512 + lane * 8;
  const u16* wb = (const u16*)(ws + WS_W2F) + ((size_t)(db * 4) * 56 + wave * 14) * 512 + lane * 8;
  f32x4 acc[4][4];
#pragma unroll
  for (int m = 0; m < 4; ++m)
#pragma unroll
    for (int n = 0; n < 4; ++n) acc[m][n] = (f32x4){0.f, 0.f, 0.f, 0.f};
  constexpr int PF = 4;
  bf16x8 A[PF][4], B[PF][4];
#pragma unroll
  for (int s = 0; s < PF; ++s)
#pragma unroll
    for (int m = 0; m < 4; ++m) {
      A[s][m] = *(const bf16x8*)(ha + ((size_t)m * 56 + s) * 512);
      B[s][m] = *(const bf16x8*)(wb + ((size_t)m * 56 + s) * 512);
    }
  __builtin_amdgcn_sched_barrier(0);
#pragma unroll
  for (int s = 0; s < 14; ++s) {
    const int sl = s % PF;
#pragma unroll
    for (int m = 0; m < 4; ++m)
#pragma unroll
      for (int n = 0; n < 4; ++n)
        acc[m][n] = __builtin_amdgcn_mfma_f32_16x16x32_bf16(A[sl][m], B[sl][n], acc[m][n], 0, 0, 0);
    __builtin_amdgcn_sched_barrier(0);
    if (s + PF < 14) {
#pragma unroll
      for (int m = 0; m < 4; ++m) {
        A[sl][m] = *(const bf16x8*)(ha + ((size_t)m * 56 + s + PF) * 512);
        B[sl][m] = *(const bf16x8*)(wb + ((size_t)m * 56 + s + PF) * 512);
      }
    }
    __builtin_amdgcn_sched_barrier(0);
  }
  STAMP(2, 1);
#pragma unroll
  for (int m = 0; m < 4; ++m)
#pragma unroll
    for (int n = 0; n < 4; ++n) red[wave][m * 4 + n][lane] = acc[m][n];
  __syncthreads();
  STAMP(2, 2);
  const int c = lane & 15, Q = lane >> 4;
  const u16* r1 = (const u16*)(ws + WS_R1T);
#pragma unroll
  for (int n = 0; n < 4; ++n) {
    f32x4 v = red[0][wave * 4 + n][lane];
#pragma unroll
    for (int w = 1; w < 4; ++w) {
      f32x4 t = red[w][wave * 4 + n][lane];
      v[0] += t[0]; v[1] += t[1]; v[2] += t[2]; v[3] += t[3];
    }
    int dcol = db * 64 + n * 16 + c;
    int tok0 = tb * 64 + wave * 16 + 4 * Q;
    uint2 rv = *(const uint2*)(r1 + (size_t)dcol * L_ + tok0);
    u32 rw[2] = {rv.x, rv.y};
#pragma unroll
    for (int r = 0; r < 4; ++r) {
      float rr = bf2f((u16)(rw[r >> 1] >> (16 * (r & 1))));
      out[(size_t)(tok0 + r) * D_ + dcol] = f2bf(rr + rbf(v[r]));
    }
  }
  STAMP(2, 3);
}

extern "C" void kernel_launch(void* const* d_in, const int* in_sizes, int n_in, void* d_out, int out_size,
                              void* d_ws, size_t ws_size, hipStream_t stream) {
  const u16* x = (const u16*)d_in[0];
  const u16* logA = (const u16*)d_in[1];
  const u16* Aim = (const u16*)d_in[2];
  const u16* Cre = (const u16*)d_in[3];
  const u16* Cim = (const u16*)d_in[4];
  const u16* logdt = (const u16*)d_in[5];
  const u16* dskip = (const u16*)d_in[6];
  const u16* wg = (const u16*)d_in[8];
  const u16* wu = (const u16*)d_in[9];
  const u16* wd = (const u16*)d_in[10];
  char* ws = (char*)d_ws;
  k1_kernel<<<K1_GRID, 256, 0, stream>>>(x, logA, Aim, Cre, Cim, logdt, wg, wu, wd, ws);
  k2_kernel<<<512, 256, 0, stream>>>(dskip, ws);
  k3_kernel<<<256, 256, 0, stream>>>(ws, (u16*)d_out);
}
```

```cpp
#include <hip/hip_runtime.h>
#include <hip/hip_bf16.h>

typedef __attribute__((ext_vector_type(8))) short bf16x8;
typedef __attribute__((ext_vector_type(4))) float f32x4;
typedef unsigned short u16;
typedef unsigned int u32;
typedef __attribute__((ext_vector_type(4))) unsigned int u32x4;

constexpr int L_ = 2048;
constexpr int D_ = 512;
constexpr int N_ = 128;
constexpr int F_ = 1792;

constexpr size_t WS_XNF = 0;
constexpr size_t WS_XNT = WS_XNF + (size_t)L_ * D_ * 2;
constexpr size_t WS_XT = WS_XNT + (size_t)L_ * D_ * 2;
constexpr size_t WS_W1F = WS_XT + (size_t)L_ * D_ * 2;
constexpr size_t WS_W2F = WS_W1F + (size_t)D_ * F_ * 2 * 2;
constexpr size_t WS_KC = WS_W2F + (size_t)D_ * F_ * 2;
constexpr size_t WS_HF = WS_KC + (size_t)D_ * 2 * L_ * 2;
constexpr size_t WS_R1T = WS_HF + (size_t)L_ * F_ * 2;
constexpr size_t WS_END = WS_R1T + (size_t)D_ * L_ * 2;

#define STAMP(kid, idx)
__device__ __forceinline__ float bf2f(u16 v) { return __uint_as_float(((u32)v) << 16); }
__device__ __forceinline__ u16 f2bf(float f) {
  __bf16 b = (__bf16)f;
  return __builtin_bit_cast(u16, b);
}
__device__ __forceinline__ float rbf(float f) { return bf2f(f2bf(f)); }
__device__ __forceinline__ u32 pack2(float lo, float hi) {
  typedef __attribute__((ext_vector_type(2))) __bf16 bf2;
  bf2 v = {(__bf16)lo, (__bf16)hi};
  return __builtin_bit_cast(u32, v);
}
__device__ __forceinline__ float lo16(u32 w) { return __uint_as_float(w << 16); }
__device__ __forceinline__ float hi16(u32 w) { return __uint_as_float(w & 0xffff0000u); }

__device__ __forceinline__ float wave_sum(float v) {
  int x = __float_as_int(v);
  v += __int_as_float(__builtin_amdgcn_update_dpp(0, x, 0xB1, 0xf, 0xf, true));
  x = __float_as_int(v);
  v += __int_as_float(__builtin_amdgcn_update_dpp(0, x, 0x4E, 0xf, 0xf, true));
  x = __float_as_int(v);
  v += __int_as_float(__builtin_amdgcn_update_dpp(0, x, 0x141, 0xf, 0xf, true));
  x = __float_as_int(v);
  v += __int_as_float(__builtin_amdgcn_update_dpp(0, x, 0x140, 0xf, 0xf, true));
  x = __float_as_int(v);
  float a = __int_as_float(__builtin_amdgcn_readlane(x, 0));
  float b = __int_as_float(__builtin_amdgcn_readlane(x, 16));
  float c = __int_as_float(__builtin_amdgcn_readlane(x, 32));
  float d = __int_as_float(__builtin_amdgcn_readlane(x, 48));
  return (a + b) + (c + d);
}

constexpr int K1_NB_KGEN = 512;
constexpr int K1_NB_LN = 128;
constexpr int K1_NB_W1 = 224;
constexpr int K1_NB_W2 = 112;
constexpr int K1_GRID = K1_NB_KGEN + K1_NB_LN + K1_NB_W1 + K1_NB_W2;

__global__ __launch_bounds__(256) void k1_kernel(
    const u16* __restrict__ x, const u16* __restrict__ logA, const u16* __restrict__ Aim,
    const u16* __restrict__ Cre, const u16* __restrict__ Cim, const u16* __restrict__ logdt,
    const u16* __restrict__ wg, const u16* __restrict__ wu, const u16* __restrict__ wd,
    char* __restrict__ ws) {
  __shared__ __attribute__((aligned(16))) char smem[64 * 136 * 2];
  const int tid = threadIdx.x;
  const int lane = tid & 63;
  const int wave = tid >> 6;
  int b = blockIdx.x;
  STAMP(0, 0);

  if (b < K1_NB_KGEN) {
    const int d = b;
    f32x4* prm = (f32x4*)smem;
    float* tab = (float*)(smem + 4096);
    if (tid < N_) {
      float dt = rbf(expf(bf2f(logdt[d])));
      float ar = rbf(expf(bf2f(logA[d * N_ + tid])));
      float zr = -ar * dt;
      float zi = bf2f(Aim[d * N_ + tid]) * dt;
      const float I_HI = 0.15915494309189535f;
      const float I_LO = 6.4206382e-9f;
      float zh = zi * I_HI;
      float zl = __builtin_fmaf(zi, I_LO, __builtin_fmaf(zi, I_HI, -zh));
      prm[2 * tid] = (f32x4){zr * 1.4426950408889634f, zh, zl, 0.f};
      float cr = bf2f(Cre[d * N_ + tid]) * dt, ci = bf2f(Cim[d * N_ + tid]) * dt;
      prm[2 * tid + 1] = (f32x4){cr, ci, -cr, -ci};
    }
    __syncthreads();
    STAMP(0, 1);
    {
      const int i = lane & 15, kk = lane >> 4;
      const float pa0 = (float)((i & 7) + 128 * (i >> 3));
      const float pa1 = (float)(i < 8 ? 256 + 128 * (i >> 2) + 2 * (i & 3) : 512 + 128 * ((i - 8) >> 1) + 4 * (i & 1));
      const float pa2 = (float)(1024 + 128 * (i > 8 ? 8 : i));
      const float pb0 = (float)(8 * i);
      f32x4 acc[3];
#pragma unroll
      for (int sc = 0; sc < 3; ++sc) acc[sc] = (f32x4){0.f, 0.f, 0.f, 0.f};
      auto cexp = [&](const f32x4& z, float pp, float& er, float& ei) {
        float e = __builtin_amdgcn_exp2f(z[0] * pp);
        float r = __builtin_rintf(z[1] * pp);
        float f = __builtin_fmaf(z[1], pp, -r);
        f = __builtin_fmaf(z[2], pp, f);
        er = e * __builtin_amdgcn_cosf(f);
        asm("" : "+v"(er));
        ei = e * __builtin_amdgcn_sinf(f);
        asm("" : "+v"(ei));
      };
      auto split = [&](float re, float im, u32& hi, u32& lo) {
        hi = pack2(re, im);
        float rl = re - __uint_as_float(hi << 16);
        float il = im - __uint_as_float(hi & 0xffff0000u);
        lo = pack2(rl, il);
      };
#pragma unroll
      for (int g = 0; g < 2; ++g) {
        u32x4 ah[3], al[3], bh, bl;
#pragma unroll
        for (int m = 0; m < 4; ++m) {
          const int n = 16 * (wave * 2 + g) + 4 * kk + m;
          f32x4 z = prm[2 * n], cc = prm[2 * n + 1];
          float er, ei;
          u32 h, l;
          cexp(z, pa0, er, ei); split(er, ei, h, l); ah[0][m] = h; al[0][m] = l;
          cexp(z, pa1, er, ei); split(er, ei, h, l); ah[1][m] = h; al[1][m] = l;
          cexp(z, pa2, er, ei); split(er, ei, h, l); ah[2][m] = h; al[2][m] = l;
          cexp(z, pb0, er, ei);
          float t0 = cc[1] * ei;
          asm("" : "+v"(t0));
          float bre = __builtin_fmaf(cc[0], er, -t0);
          float t1 = cc[2] * ei;
          asm("" : "+v"(t1));
          float bim = __builtin_fmaf(cc[3], er, t1);
          split(bre, bim, h, l); bh[m] = h; bl[m] = l;
        }
#pragma unroll
        for (int sc = 0; sc < 3; ++sc) {
          acc[sc] = __builtin_amdgcn_mfma_f32_16x16x32_bf16(__builtin_bit_cast(bf16x8, al[sc]), __builtin_bit_cast(bf16x8, bh), acc[sc], 0, 0, 0);
          acc[sc] = __builtin_amdgcn_mfma_f32_16x16x32_bf16(__builtin_bit_cast(bf16x8, ah[sc]), __builtin_bit_cast(bf16x8, bl), acc[sc], 0, 0, 0);
          acc[sc] = __builtin_amdgcn_mfma_f32_16x16x32_bf16(__builtin_bit_cast(bf16x8, ah[sc]), __builtin_bit_cast(bf16x8, bh), acc[sc], 0, 0, 0);
        }
      }
      float* tw = tab + wave * 768;
#pragma unroll
      for (int r = 0; r < 4; ++r) {
        int row = 4 * kk + r;
#pragma unroll
        for (int sc = 0; sc < 3; ++sc) tw[sc * 256 + 16 * row + i] = acc[sc][r];
      }
    }
    __syncthreads();
    STAMP(0, 2);
    {
      u16* kc = (u16*)(ws + WS_KC) + (size_t)d * 2 * L_;
      u32x4 hi, lo;
#pragma unroll
      for (int e2 = 0; e2 < 4; ++e2) {
        float kv[2];
#pragma unroll
        for (int e1 = 0; e1 < 2; ++e1) {
          int o = tid * 8 + 2 * e2 + e1;
          int l = (o & ~63) + 63 - (o & 63);
          int p = (int)rbf((float)l);
          int j = (p & 127) >> 3;
          int row;
          if (p < 256) row = (p & 7) + 8 * (p >> 7);
          else if (p < 512) row = 16 + (((p - 256) >> 7) << 2) + ((p & 7) >> 1);
          else if (p < 1024) row = 24 + (((p - 512) >> 7) << 1) + ((p & 7) >> 2);
          else row = 32 + ((p - 1024) >> 7);
          int idx = row * 16 + j;
          kv[e1] = 2.0f * ((tab[idx] + tab[768 + idx]) + (tab[2 * 768 + idx] + tab[3 * 768 + idx]));
        }
        hi[e2] = pack2(kv[0], kv[1]);
        lo[e2] = pack2(kv[0] - lo16(hi[e2]), kv[1] - hi16(hi[e2]));
      }
      *(u32x4*)(kc + tid * 8) = hi;
      *(u32x4*)(kc + L_ + tid * 8) = lo;
    }
    STAMP(0, 3);
    return;
  }
  b -= K1_NB_KGEN;
  __builtin_amdgcn_s_setprio(3);
  if (b < K1_NB_LN) {
    constexpr int LDW = 520;
    u16* sn = (u16*)smem;
    const int row0 = b * 16;
    u32x4 v[4];
#pragma unroll
    for (int r = 0; r < 4; ++r) v[r] = *(const u32x4*)(x + (size_t)(row0 + wave * 4 + r) * D_ + lane * 8);
#pragma unroll
    for (int r = 0; r < 4; ++r) {
      float f[8];
#pragma unroll
      for (int e = 0; e < 4; ++e) { f[2 * e] = bf2f((u16)(v[r][e] & 0xffff)); f[2 * e + 1] = bf2f((u16)(v[r][e] >> 16)); }
      float s = 0.f;
#pragma unroll
      for (int e = 0; e < 8; ++e) s += f[e];
      s = wave_sum(s);
      float mean = s * (1.0f / D_);
      float q = 0.f;
#pragma unroll
      for (int e = 0; e < 8; ++e) { float dd = f[e] - mean; q += dd * dd; }
      q = wave_sum(q);
      float var = q * (1.0f / D_);
      float mb = rbf(mean);
      float vb = rbf(var);
      float den = rbf(sqrtf(rbf(vb + 1e-5f)));
      float rinv = 1.0f / den;
      u32x4 ow;
#pragma unroll
      for (int e = 0; e < 4; ++e) {
        u32 dp = pack2(f[2 * e] - mb, f[2 * e + 1] - mb);
        ow[e] = pack2(lo16(dp) * rinv, hi16(dp) * rinv);
      }
      int lr = wave * 4 + r;
      *(u32x4*)(sn + lr * LDW + lane * 8) = ow;
    }
    STAMP(0, 1);
    __syncthreads();
    STAMP(0, 2);
    u16* xnf = (u16*)(ws + WS_XNF) + (size_t)b * 16 * 512;
#pragma unroll
    for (int it = 0; it < 4; ++it) {
      int pc = it * 256 + tid;
      int ln = pc & 63, ks = pc >> 6;
      int c = ln & 15, q = ln >> 4;
      u32x4 val = *(const u32x4*)(sn + c * LDW + ks * 32 + q * 8);
      *(u32x4*)(xnf + (size_t)pc * 8) = val;
    }
    u16* xnt = (u16*)(ws + WS_XNT);
    {
      const u16* sp = sn;
      u16* dp = xnt;
      u32 w[16];
#pragma unroll
      for (int r = 0; r < 16; ++r) w[r] = *(const u32*)(sp + r * LDW + 2 * tid);
      u32x4 lo0, lo1, hi0, hi1;
#pragma unroll
      for (int e = 0; e < 4; ++e) {
        lo0[e] = __builtin_amdgcn_perm(w[2 * e + 1], w[2 * e], 0x05040100);
        hi0[e] = __builtin_amdgcn_perm(w[2 * e + 1], w[2 * e], 0x07060302);
        lo1[e] = __builtin_amdgcn_perm(w[8 + 2 * e + 1], w[8 + 2 * e], 0x05040100);
        hi1[e] = __builtin_amdgcn_perm(w[8 + 2 * e + 1], w[8 + 2 * e], 0x07060302);
      }
      u16* d0 = dp + (size_t)(2 * tid) * L_ + row0;
      *(u32x4*)(d0) = lo0;
      *(u32x4*)(d0 + 8) = lo1;
      *(u32x4*)(d0 + L_) = hi0;
      *(u32x4*)(d0 + L_ + 8) = hi1;
    }
    STAMP(0, 3);
    return;
  }
  b -= K1_NB_LN;
  {
    constexpr int LDW = 136;
    u16* st = (u16*)smem;
    const u16* src;
    int ld, ks0, nch;
    int kind;
    if (b < K1_NB_W1) {
      kind = b / 112;
      int r = b % 112;
      ks0 = (r / 14) * 2; nch = r % 14;
      src = kind == 0 ? wg : wu;
      ld = F_;
    } else {
      int r = b - K1_NB_W1;
      kind = 2;
      ks0 = (r >> 2) * 2; nch = r & 3;
      src = wd;
      ld = D_;
    }
    u32x4 val[4];
#pragma unroll
    for (int h = 0; h < 4; ++h) {
      int r = (tid >> 4) + 16 * h, cc = (tid & 15) * 8;
      val[h] = *(const u32x4*)(src + (size_t)(ks0 * 32 + r) * ld + nch * 128 + cc);
    }
#pragma unroll
    for (int h = 0; h < 4; ++h) {
      int r = (tid >> 4) + 16 * h, cc = (tid & 15) * 8;
      *(u32x4*)(st + r * LDW + cc) = val[h];
    }
    __syncthreads();
#pragma unroll
    for (int h = 0; h < 4; ++h) {
      int pc = (h & 1) * 256 + tid;
      int ks = ks0 + (h >> 1);
      int ln = pc & 63, nt = pc >> 6;
      int a = ln & 15, q = ln >> 4;
      const u16* sr = st + ((h >> 1) * 32 + 8 * q) * LDW + nt * 16 + a;
      u32x4 w;
#pragma unroll
      for (int e = 0; e < 4; ++e) w[e] = (u32)sr[(2 * e) * LDW] | ((u32)sr[(2 * e + 1) * LDW] << 16);
      int ft = nch * 8 + nt;
      u16* dst;
      if (kind < 2) {
        int fg = ft / 7, tau = ft % 7 + 7 * kind;
        dst = (u16*)(ws + WS_W1F) + ((size_t)(fg * 16 + ks) * 14 + tau) * 512 + ln * 8;
      } else {
        dst = (u16*)(ws + WS_W2F) + ((size_t)ft * 56 + ks) * 512 + ln * 8;
      }
      *(u32x4*)dst = w;
    }
  }
}

constexpr int CV_UCOPY = 4416;
constexpr int CV_KROW = 160;
constexpr int CV_KARR = 6400;
constexpr int CV_CH = 4 * CV_UCOPY + 2 * CV_KARR;
constexpr int G1_STAGE = 22 * 1024;
constexpr int K2_LDS = 3 * G1_STAGE;

typedef __attribute__((ext_vector_type(2))) unsigned int u32x2;
#define MF(acc, A, B) acc = __builtin_amdgcn_mfma_f32_16x16x32_bf16(A, B, acc, 0, 0, 0)
template <int OFF> __device__ __forceinline__ u32x2 dsr64(u32 addr) {
  u32x2 r;
  asm volatile("ds_read_b64 %0, %1 offset:%2" : "=v"(r) : "v"(addr), "i"(OFF));
  return r;
}
template <int OFF> __device__ __forceinline__ bf16x8 dsr128(u32 addr) {
  bf16x8 r;
  asm volatile("ds_read_b128 %0, %1 offset:%2" : "=v"(r) : "v"(addr), "i"(OFF));
  return r;
}
template <int M> __device__ __forceinline__ bf16x8 ldW(u32 aaddr) {
  u32x2 x0 = dsr64<M * 32>(aaddr);
  u32x2 x1 = dsr64<M * 32 + 8>(aaddr);
  u32x4 v = {x0[0], x0[1], x1[0], x1[1]};
  return __builtin_bit_cast(bf16x8, v);
}
template <int N> __device__ __forceinline__ void wait_lgkm() {
  asm volatile("s_waitcnt lgkmcnt(%0)" ::"i"(N) : "memory");
  __builtin_amdgcn_sched_barrier(0);
}
struct ConvStep { bf16x8 w2, w3, w4, w5, h0, h1, l0, l1; };
template <int DL, int NDL> __device__ __forceinline__ void conv_load(ConvStep& st, u32 aaddr, u32 haddr, u32 laddr) {
  st.w2 = ldW<4 * DL + 2>(aaddr); st.w3 = ldW<4 * DL + 3>(aaddr);
  st.w4 = ldW<4 * DL + 4>(aaddr); st.w5 = ldW<4 * DL + 5>(aaddr);
  st.h0 = dsr128<(31 - DL) * CV_KROW>(haddr); st.h1 = dsr128<(31 - DL) * CV_KROW + 64>(haddr);
  if constexpr (DL < NDL) {
    st.l0 = dsr128<(31 - DL) * CV_KROW>(laddr); st.l1 = dsr128<(31 - DL) * CV_KROW + 64>(laddr);
  }
}
#define SB __builtin_amdgcn_sched_barrier(0)
template <int DL, int NDH, int NDL>
__device__ __forceinline__ void conv_step(ConvStep (&st)[3], bf16x8& w0, bf16x8& w1, u32 aaddr, u32 haddr, u32 laddr,
                                          f32x4 (&accH)[4], f32x4 (&accL)[4]) {
  if constexpr (DL < NDH) {
    constexpr int CN1 = (DL + 1 < NDH) ? ((DL + 1 < NDL) ? 12 : 10) : 0;
    constexpr bool PF = (DL + 2 < NDH);
    constexpr int DN = DL + 2;
    wait_lgkm<CN1>();
    ConvStep& c = st[DL % 3];
    ConvStep& n = st[(DL + 2) % 3];
    if constexpr (DL < NDL) {
      MF(accH[0], w0, c.h0); SB; if constexpr (PF) n.w2 = ldW<4 * DN + 2>(aaddr); SB;
      MF(accH[1], w1, c.h0); MF(accH[2], c.w2, c.h0); SB; if constexpr (PF) n.w3 = ldW<4 * DN + 3>(aaddr); SB;
      MF(accH[3], c.w3, c.h0); MF(accL[0], w0, c.l0); SB; if constexpr (PF) n.w4 = ldW<4 * DN + 4>(aaddr); SB;
      MF(accL[1], w1, c.l0); MF(accL[2], c.w2, c.l0); SB; if constexpr (PF) n.w5 = ldW<4 * DN + 5>(aaddr); SB;
      MF(accL[3], c.w3, c.l0); MF(accH[0], c.w2, c.h1); SB; if constexpr (PF) n.h0 = dsr128<(31 - DN) * CV_KROW>(haddr); SB;
      MF(accH[1], c.w3, c.h1); MF(accH[2], c.w4, c.h1); SB; if constexpr (PF) n.h1 = dsr128<(31 - DN) * CV_KROW + 64>(haddr); SB;
      MF(accH[3], c.w5, c.h1); MF(accL[0], c.w2, c.l1); SB; if constexpr (PF && DN < NDL) n.l0 = dsr128<(31 - DN) * CV_KROW>(laddr); SB;
      MF(accL[1], c.w3, c.l1); MF(accL[2], c.w4, c.l1); SB; if constexpr (PF && DN < NDL) n.l1 = dsr128<(31 - DN) * CV_KROW + 64>(laddr); SB;
      MF(accL[3], c.w5, c.l1); SB;
    } else {
      MF(accH[0], w0, c.h0); SB; if constexpr (PF) n.w2 = ldW<4 * DN + 2>(aaddr); SB;
      MF(accH[1], w1, c.h0); SB; if constexpr (PF) n.w3 = ldW<4 * DN + 3>(aaddr); SB;
      MF(accH[2], c.w2, c.h0); SB; if constexpr (PF) n.w4 = ldW<4 * DN + 4>(aaddr); SB;
      MF(accH[3], c.w3, c.h0); SB; if constexpr (PF) n.w5 = ldW<4 * DN + 5>(aaddr); SB;
      MF(accH[0], c.w2, c.h1); SB; if constexpr (PF) n.h0 = dsr128<(31 - DN) * CV_KROW>(haddr); SB;
      MF(accH[1], c.w3, c.h1); SB; if constexpr (PF) n.h1 = dsr128<(31 - DN) * CV_KROW + 64>(haddr); SB;
      MF(accH[2], c.w4, c.h1); MF(accH[3], c.w5, c.h1); SB;
    }
    w0 = c.w4; w1 = c.w5;
    conv_step<DL + 1, NDH, NDL>(st, w0, w1, aaddr, haddr, laddr, accH, accL);
  }
}
template <int NDH, int NDL>
__device__ __forceinline__ void conv_main(u32 aaddr, u32 haddr, u32 laddr, f32x4 (&accH)[4], f32x4 (&accL)[4]) {
  ConvStep st[3];
  bf16x8 w0 = ldW<0>(aaddr), w1 = ldW<1>(aaddr);
  conv_load<0, NDL>(st[0], aaddr, haddr, laddr);
  conv_load<1, NDL>(st[1], aaddr, haddr, laddr);
  conv_step<0, NDH, NDL>(st, w0, w1, aaddr, haddr, laddr, accH, accL);
}

__device__ __forceinline__ float gelu_tanh(float g) {
  const float c0 = 2.0f * 0.7978845608028654f * 1.4426950408889634f;
  const float c1 = c0 * 0.044715f;
  float g2 = g * g;
  float z = g * __builtin_fmaf(g2, c1, c0);
  float e = __builtin_amdgcn_exp2f(z);
  float r = __builtin_amdgcn_rcpf(1.0f + e);
  return __builtin_fmaf(-g, r, g);
}

__global__ __launch_bounds__(256, 2) void k2_kernel(const u16* __restrict__ dskip, char* __restrict__ ws) {
  __shared__ __attribute__((aligned(16))) char smem[K2_LDS];
  const int tid = threadIdx.x;
  const int lane = tid & 63;
  const int wave = __builtin_amdgcn_readfirstlane(tid >> 6);
  const int b = blockIdx.x;
  STAMP(1, 0);

  if (b < 256) {
    const int tt = ((b & 7) >> 2) * 8 + (b >> 5);
    const int fg = (b & 3) * 4 + ((b >> 3) & 3);
    const u16* w1 = (const u16*)(ws + WS_W1F) + (size_t)fg * 16 * 14 * 512;
    const u16* xf = (const u16*)(ws + WS_XNF) + (size_t)(tt * 8) * 16 * 512;
    f32x4 acc[14][2];
#pragma unroll
    for (int i = 0; i < 14; ++i) { acc[i][0] = (f32x4){0.f, 0.f, 0.f, 0.f}; acc[i][1] = (f32x4){0.f, 0.f, 0.f, 0.f}; }
    const u16* gsrc[6];
    int gdst[6];
    int gstep[6];
#pragma unroll
    for (int i = 0; i < 6; ++i) {
      int p = wave + 4 * i;
      if (p >= 22) p -= 22;
      if (p < 14) { gsrc[i] = w1 + p * 512 + lane * 8; gstep[i] = 14 * 512; }
      else { gsrc[i] = xf + (p - 14) * 16 * 512 + lane * 8; gstep[i] = 512; }
      gdst[i] = p * 1024 + lane * 16;
    }
    const int rot = 0;
    auto issue1 = [&](int ks, int i) {
      char* sb = smem + (ks % 3) * G1_STAGE;
      __builtin_amdgcn_global_load_lds((const __attribute__((address_space(1))) u32*)(gsrc[i] + ((ks + rot) & 15) * gstep[i]),
                                       (__attribute__((address_space(3))) u32*)(sb + gdst[i]), 16, 0, 0);
    };
#pragma unroll
    for (int i = 0; i < 6; ++i) issue1(0, i);
#pragma unroll
    for (int i = 0; i < 6; ++i) issue1(1, i);
#pragma unroll
    for (int i = 0; i < 6; ++i) issue1(2, i);
    asm volatile("s_waitcnt vmcnt(12)" ::: "memory");
    __builtin_amdgcn_s_barrier();
    bf16x8 alo[7], ahi[7], t0, t1;
    {
      const char* buf = smem + lane * 16;
      t0 = *(const bf16x8*)(buf + (14 + 2 * wave) * 1024);
      t1 = *(const bf16x8*)(buf + (15 + 2 * wave) * 1024);
#pragma unroll
      for (int t = 0; t < 7; ++t) alo[t] = *(const bf16x8*)(buf + t * 1024);
    }
#pragma unroll
    for (int ks = 0; ks < 16; ++ks) {
      const char* buf = smem + (ks % 3) * G1_STAGE + lane * 16;
#pragma unroll
      for (int t = 0; t < 7; ++t) {
        acc[t][0] = __builtin_amdgcn_mfma_f32_16x16x32_bf16(alo[t], t0, acc[t][0], 0, 0, 0);
        acc[t][1] = __builtin_amdgcn_mfma_f32_16x16x32_bf16(alo[t], t1, acc[t][1], 0, 0, 0);
        __builtin_amdgcn_sched_barrier(0);
        ahi[t] = *(const bf16x8*)(buf + (7 + t) * 1024);
        __builtin_amdgcn_sched_barrier(0);
      }
      bf16x8 n0 = t0, n1 = t1;
      if (ks < 15) {
        if (ks < 14) asm volatile("s_waitcnt vmcnt(6) lgkmcnt(0)" ::: "memory");
        else asm volatile("s_waitcnt vmcnt(0) lgkmcnt(0)" ::: "memory");
        __builtin_amdgcn_s_barrier();
        __builtin_amdgcn_sched_barrier(0);
      }
      const char* nb = smem + ((ks + 1) % 3) * G1_STAGE + lane * 16;
#pragma unroll
      for (int t = 0; t < 7; ++t) {
        acc[7 + t][0] = __builtin_amdgcn_mfma_f32_16x16x32_bf16(ahi[t], t0, acc[7 + t][0], 0, 0, 0);
        __builtin_amdgcn_sched_barrier(0);
        if (ks < 15) {
          alo[t] = *(const bf16x8*)(nb + t * 1024);
          if (t == 0) n0 = *(const bf16x8*)(nb + (14 + 2 * wave) * 1024);
          if (t == 1) n1 = *(const bf16x8*)(nb + (15 + 2 * wave) * 1024);
          __builtin_amdgcn_sched_barrier(0);
        }
        acc[7 + t][1] = __builtin_amdgcn_mfma_f32_16x16x32_bf16(ahi[t], t1, acc[7 + t][1], 0, 0, 0);
        if (t < 6 && ks + 3 < 16) {
          __builtin_amdgcn_sched_barrier(0);
          issue1(ks + 3, t);
        }
        __builtin_amdgcn_sched_barrier(0);
      }
      t0 = n0; t1 = n1;
    }
    STAMP(1, 2);
    u16* hf = (u16*)(ws + WS_HF);
    const int c = lane & 15, Q = lane >> 4;
#pragma unroll
    for (int t = 0; t < 7; ++t) {
      int ft = fg * 7 + t;
      int ks2 = ft >> 1;
      int q = 2 * (ft & 1) + (Q >> 1);
#pragma unroll
      for (int m = 0; m < 2; ++m) {
        float hv[4];
#pragma unroll
        for (int r = 0; r < 4; ++r) {
          float g = rbf(acc[t][m][r]);
          float u = rbf(acc[t + 7][m][r]);
          hv[r] = rbf(gelu_tanh(g)) * u;
        }
        int tile = tt * 8 + 2 * wave + m;
        uint2 o = make_uint2(pack2(hv[0], hv[1]), pack2(hv[2], hv[3]));
        *(uint2*)(hf + (((size_t)tile * 56 + ks2) * 64 + (c + 16 * q)) * 8 + 4 * (Q & 1)) = o;
      }
    }
    STAMP(1, 3);
    return;
  }

  {
    const int chl = wave >> 1;
    const int d = (b - 256) * 2 + chl;
    const int t128 = tid & 127;
    char* cb = smem + chl * CV_CH;
    const u16* ut = (const u16*)(ws + WS_XNT) + (size_t)d * L_;
    const u16* kc = (const u16*)(ws + WS_KC) + (size_t)d * 2 * L_;
    char* kb = cb + 4 * CV_UCOPY;
    {
      u32x4 v0[3], v1[3], kv[4];
#pragma unroll
      for (int it = 0; it < 3; ++it) {
        int cch = it * 128 + t128 - 8;
        int c0 = cch < 0 ? 0 : (cch > 255 ? 255 : cch);
        int c1 = cch + 1 < 0 ? 0 : (cch + 1 > 255 ? 255 : cch + 1);
        v0[it] = *(const u32x4*)(ut + c0 * 8);
        v1[it] = *(const u32x4*)(ut + c1 * 8);
      }
#pragma unroll
      for (int it = 0; it < 4; ++it) {
        int pc = it * 128 + t128;
        kv[it] = *(const u32x4*)(kc + pc * 8);
      }
      __builtin_amdgcn_sched_barrier(0);
#pragma unroll
      for (int it = 0; it < 3; ++it) {
        int X = it * 128 + t128;
        int cch = X - 8;
        bool ok0 = (cch >= 0 && cch < 256), ok1 = (cch + 1 >= 0 && cch + 1 < 256);
        u32 w[6] = {ok0 ? v0[it][0] : 0u, ok0 ? v0[it][1] : 0u, ok0 ? v0[it][2] : 0u, ok0 ? v0[it][3] : 0u,
                    ok1 ? v1[it][0] : 0u, ok1 ? v1[it][1] : 0u};
        u32 s1[5];
#pragma unroll
        for (int e = 0; e < 5; ++e) s1[e] = __builtin_amdgcn_alignbit(w[e + 1], w[e], 16);
        if (X < 276) {
          *(u32x4*)(cb + 0 * CV_UCOPY + X * 16) = (u32x4){w[0], w[1], w[2], w[3]};
          *(u32x4*)(cb + 1 * CV_UCOPY + X * 16) = (u32x4){s1[0], s1[1], s1[2], s1[3]};
          *(u32x4*)(cb + 2 * CV_UCOPY + X * 16) = (u32x4){w[1], w[2], w[3], w[4]};
          *(u32x4*)(cb + 3 * CV_UCOPY + X * 16) = (u32x4){s1[1], s1[2], s1[3], s1[4]};
        }
      }
#pragma unroll
      for (int it = 0; it < 4; ++it) {
        int pc = it * 128 + t128;
        int arr = pc >> 8, row = (pc >> 3) & 31, ch = pc & 7;
        *(u32x4*)(kb + arr * CV_KARR + (row + 7) * CV_KROW + ch * 16) = kv[it];
      }
    }
    if (t128 < 112) {
      int arr = t128 / 56, r = t128 % 56;
      *(uint4*)(kb + arr * CV_KARR + (r >> 3) * CV_KROW + (r & 7) * 16) = make_uint4(0, 0, 0, 0);
    }
    __syncthreads();
    STAMP(1, 1);

    const int p = wave & 1;
    const int gH = p ? 2 : 3, gL = p ? 1 : 0;
    const int a = lane & 15, q = lane >> 4;
    const char* abase = cb + ((a + 1) & 3) * CV_UCOPY + (8 * q + 4 * ((a + 1) >> 2)) * 2;
    const char* bH = kb + (a >= 8 ? CV_KARR : 0) + (8 * gH + (a & 7) + 7) * CV_KROW + 16 * q;
    const char* bL = kb + (a >= 8 ? CV_KARR : 0) + (8 * gL + (a & 7) + 7) * CV_KROW + 16 * q;
    f32x4 accH[4], accL[4];
#pragma unroll
    for (int i = 0; i < 4; ++i) { accH[i] = (f32x4){0.f, 0.f, 0.f, 0.f}; accL[i] = (f32x4){0.f, 0.f, 0.f, 0.f}; }
    const int col = (a < 8) ? (8 * gH + a) : (8 * gL + a - 8);
    u32x2 uvp[4];
#pragma unroll
    for (int rt = 0; rt < 4; ++rt) {
      int t0 = 64 * col + 16 * rt + 4 * q;
      uvp[rt] = *(const u32x2*)(ut + t0);
    }
    const float dk = bf2f(dskip[d]);
    __builtin_amdgcn_sched_barrier(0);
    {
      typedef __attribute__((address_space(3))) const char* lptr;
      u32 aaddr = (u32)(size_t)(lptr)abase;
      u32 haddr = (u32)(size_t)(lptr)bH - 31 * CV_KROW;
      u32 laddr = (u32)(size_t)(lptr)bL - 31 * CV_KROW;
      if (p) conv_main<24, 16>(aaddr, haddr, laddr, accH, accL);
      else conv_main<32, 8>(aaddr, haddr, laddr, accH, accL);
    }
    STAMP(1, 2);
    u16* r1 = (u16*)(ws + WS_R1T) + (size_t)d * L_;
#pragma unroll
    for (int rt = 0; rt < 4; ++rt) {
      int t0 = 64 * col + 16 * rt + 4 * q;
      float o[4];
#pragma unroll
      for (int r = 0; r < 4; ++r) {
        float mine = (a < 8) ? accH[rt][r] : accL[rt][r];
        float give = (a < 8) ? accL[rt][r] : accH[rt][r];
        float other = __int_as_float(__builtin_amdgcn_update_dpp(0, __float_as_int(give), 0x128, 0xf, 0xf, true));
        float y = rbf(mine + other);
        float un = bf2f((u16)(uvp[rt][r >> 1] >> (16 * (r & 1))));
        o[r] = y + rbf(un * dk);
      }
      *(uint2*)(r1 + t0) = make_uint2(pack2(o[0], o[1]), pack2(o[2], o[3]));
    }
    STAMP(1, 3);
  }
}

__global__ __launch_bounds__(256) void k3_kernel(const u16* __restrict__ x, char* __restrict__ ws, u16* __restrict__ out) {
  __shared__ __attribute__((aligned(16))) f32x4 red[4][16][64];
  const int tid = threadIdx.x;
  const int lane = tid & 63;
  const int wave = __builtin_amdgcn_readfirstlane(tid >> 6);
  const int b = blockIdx.x;
  STAMP(2, 0);
  const int tb = (b & 7) * 4 + ((b >> 3) & 3);
  const int db = b >> 5;
  const u16* ha = (const u16*)(ws + WS_HF) + ((size_t)(tb * 4) * 56 + wave * 14) * 512 + lane * 8;
  const u16* wb = (const u16*)(ws + WS_W2F) + ((size_t)(db * 4) * 56 + wave * 14) * 512 + lane * 8;
  f32x4 acc[4][4];
#pragma unroll
  for (int m = 0; m < 4; ++m)
#pragma unroll
    for (int n = 0; n < 4; ++n) acc[m][n] = (f32x4){0.f, 0.f, 0.f, 0.f};
  constexpr int PF = 3;
  bf16x8 A[PF][4], B[PF][4];
#pragma unroll
  for (int s = 0; s < PF; ++s)
#pragma unroll
    for (int m = 0; m < 4; ++m) {
      A[s][m] = *(const bf16x8*)(ha + ((size_t)m * 56 + s) * 512);
      B[s][m] = *(const bf16x8*)(wb + ((size_t)m * 56 + s) * 512);
    }
  __builtin_amdgcn_sched_barrier(0);
#pragma unroll
  for (int s = 0; s < 14; ++s) {
    const int sl = s % PF;
#pragma unroll
    for (int m = 0; m < 4; ++m)
#pragma unroll
      for (int n = 0; n < 4; ++n)
        acc[m][n] = __builtin_amdgcn_mfma_f32_16x16x32_bf16(A[sl][m], B[sl][n], acc[m][n], 0, 0, 0);
    __builtin_amdgcn_sched_barrier(0);
    if (s + PF < 14) {
#pragma unroll
      for (int m = 0; m < 4; ++m) {
        A[sl][m] = *(const bf16x8*)(ha + ((size_t)m * 56 + s + PF) * 512);
        B[sl][m] = *(const bf16x8*)(wb + ((size_t)m * 56 + s + PF) * 512);
      }
    }
    __builtin_amdgcn_sched_barrier(0);
  }
  STAMP(2, 1);
  const int c = lane & 15, Q = lane >> 4;
  const u16* at = (const u16*)(ws + WS_R1T);
  const int tok0 = tb * 64 + wave * 16 + 4 * Q;
  u32x2 av[4];
  u16 xv[4][4];
#pragma unroll
  for (int n = 0; n < 4; ++n) {
    int dcol = db * 64 + n * 16 + c;
    av[n] = *(const u32x2*)(at + (size_t)dcol * L_ + tok0);
#pragma unroll
    for (int r = 0; r < 4; ++r) xv[n][r] = x[(size_t)(tok0 + r) * D_ + dcol];
  }
#pragma unroll
  for (int m = 0; m < 4; ++m)
#pragma unroll
    for (int n = 0; n < 4; ++n) red[wave][m * 4 + n][lane] = acc[m][n];
  __syncthreads();
  STAMP(2, 2);
#pragma unroll
  for (int n = 0; n < 4; ++n) {
    f32x4 v = red[0][wave * 4 + n][lane];
#pragma unroll
    for (int w = 1; w < 4; ++w) {
      f32x4 t = red[w][wave * 4 + n][lane];
      v[0] += t[0]; v[1] += t[1]; v[2] += t[2]; v[3] += t[3];
    }
    int dcol = db * 64 + n * 16 + c;
#pragma unroll
    for (int r = 0; r < 4; ++r) {
      float aa = (r & 1) ? hi16(av[n][r >> 1]) : lo16(av[n][r >> 1]);
      float r1 = rbf(bf2f(xv[n][r]) + aa);
      out[(size_t)(tok0 + r) * D_ + dcol] = f2bf(r1 + rbf(v[r]));
    }
  }
  STAMP(2, 3);
}

extern "C" void kernel_launch(void* const* d_in, const int* in_sizes, int n_in, void* d_out, int out_size,
                              void* d_ws, size_t ws_size, hipStream_t stream) {
  const u16* x = (const u16*)d_in[0];
  const u16* logA = (const u16*)d_in[1];
  const u16* Aim = (const u16*)d_in[2];
  const u16* Cre = (const u16*)d_in[3];
  const u16* Cim = (const u16*)d_in[4];
  const u16* logdt = (const u16*)d_in[5];
  const u16* dskip = (const u16*)d_in[6];
  const u16* wg = (const u16*)d_in[8];
  const u16* wu = (const u16*)d_in[9];
  const u16* wd = (const u16*)d_in[10];
  char* ws = (char*)d_ws;
  k1_kernel<<<K1_GRID, 256, 0, stream>>>(x, logA, Aim, Cre, Cim, logdt, wg, wu, wd, ws);
  k2_kernel<<<512, 256, 0, stream>>>(dskip, ws);
  k3_kernel<<<256, 256, 0, stream>>>(x, ws, (u16*)d_out);
}
```

```cpp
#include <hip/hip_runtime.h>
#include <hip/hip_bf16.h>

typedef __attribute__((ext_vector_type(8))) short bf16x8;
typedef __attribute__((ext_vector_type(4))) float f32x4;
typedef unsigned short u16;
typedef unsigned int u32;
typedef __attribute__((ext_vector_type(4))) unsigned int u32x4;
typedef __attribute__((ext_vector_type(2))) unsigned int u32x2;

constexpr int L_ = 2048;
constexpr int D_ = 512;
constexpr int N_ = 128;
constexpr int F_ = 1792;

constexpr size_t WS_XNF = 0;
constexpr size_t WS_XNT = WS_XNF + (size_t)L_ * D_ * 2;
constexpr size_t WS_XT = WS_XNT + (size_t)L_ * D_ * 2;
constexpr size_t WS_W1F = WS_XT + (size_t)L_ * D_ * 2;
constexpr size_t WS_W2F = WS_W1F + (size_t)D_ * F_ * 2 * 2;
constexpr size_t WS_KC = WS_W2F + (size_t)D_ * F_ * 2;
constexpr size_t WS_HF = WS_KC + (size_t)D_ * 2 * L_ * 2;
constexpr size_t WS_R1T = WS_HF + (size_t)L_ * F_ * 2;
constexpr size_t WS_END = WS_R1T + (size_t)D_ * L_ * 2;

#define STAMP(kid, idx)
#define GST(T, p, v) (*(T*)(p) = (v))
template <typename T> __device__ __forceinline__ void gst_sc1(T* p, T v);
template <> __device__ __forceinline__ void gst_sc1<u32x4>(u32x4* p, u32x4 v) { asm volatile("global_store_dwordx4 %0, %1, off sc1\n\ts_nop 1" :: "v"(p), "v"(v) : "memory"); }
template <> __device__ __forceinline__ void gst_sc1<u32x2>(u32x2* p, u32x2 v) { asm volatile("global_store_dwordx2 %0, %1, off sc1\n\ts_nop 1" :: "v"(p), "v"(v) : "memory"); }
#define GST(T, p, v) gst_sc1<T>((T*)(p), (T)(v))
#define GST2 GST
__device__ __forceinline__ float bf2f(u16 v) { return __uint_as_float(((u32)v) << 16); }
__device__ __forceinline__ u16 f2bf(float f) {
  __bf16 b = (__bf16)f;
  return __builtin_bit_cast(u16, b);
}
__device__ __forceinline__ float rbf(float f) { return bf2f(f2bf(f)); }
__device__ __forceinline__ u32 pack2(float lo, float hi) {
  typedef __attribute__((ext_vector_type(2))) __bf16 bf2;
  bf2 v = {(__bf16)lo, (__bf16)hi};
  return __builtin_bit_cast(u32, v);
}
__device__ __forceinline__ float lo16(u32 w) { return __uint_as_float(w << 16); }
__device__ __forceinline__ float hi16(u32 w) { return __uint_as_float(w & 0xffff0000u); }

__device__ __forceinline__ float wave_sum(float v) {
  int x = __float_as_int(v);
  v += __int_as_float(__builtin_amdgcn_update_dpp(0, x, 0xB1, 0xf, 0xf, true));
  x = __float_as_int(v);
  v += __int_as_float(__builtin_amdgcn_update_dpp(0, x, 0x4E, 0xf, 0xf, true));
  x = __float_as_int(v);
  v += __int_as_float(__builtin_amdgcn_update_dpp(0, x, 0x141, 0xf, 0xf, true));
  x = __float_as_int(v);
  v += __int_as_float(__builtin_amdgcn_update_dpp(0, x, 0x140, 0xf, 0xf, true));
  x = __float_as_int(v);
  float a = __int_as_float(__builtin_amdgcn_readlane(x, 0));
  float b = __int_as_float(__builtin_amdgcn_readlane(x, 16));
  float c = __int_as_float(__builtin_amdgcn_readlane(x, 32));
  float d = __int_as_float(__builtin_amdgcn_readlane(x, 48));
  return (a + b) + (c + d);
}

constexpr int K1_NB_KGEN = 512;
constexpr int K1_NB_LN = 128;
constexpr int K1_NB_W1 = 112;
constexpr int K1_NB_W2 = 56;
constexpr int K1_GRID = K1_NB_KGEN + K1_NB_LN + K1_NB_W1 + K1_NB_W2;

__global__ __launch_bounds__(512) void k1_kernel(
    const u16* __restrict__ x, const u16* __restrict__ logA, const u16* __restrict__ Aim,
    const u16* __restrict__ Cre, const u16* __restrict__ Cim, const u16* __restrict__ logdt,
    const u16* __restrict__ wg, const u16* __restrict__ wu, const u16* __restrict__ wd,
    char* __restrict__ ws) {
  __shared__ __attribute__((aligned(16))) char smem[128 * 136 * 2];
  const int tid = threadIdx.x;
  const int lane = tid & 63;
  const int wave = tid >> 6;
  int b = blockIdx.x;
  STAMP(0, 0);

  if (b < K1_NB_KGEN) {
    const int d = b;
    f32x4* prm = (f32x4*)smem;
    float* tab = (float*)(smem + 4096);
    if (tid < N_) {
      float dt = rbf(expf(bf2f(logdt[d])));
      float ar = rbf(expf(bf2f(logA[d * N_ + tid])));
      float zr = -ar * dt;
      float zi = bf2f(Aim[d * N_ + tid]) * dt;
      const float I_HI = 0.15915494309189535f;
      const float I_LO = 6.4206382e-9f;
      float zh = zi * I_HI;
      float zl = __builtin_fmaf(zi, I_LO, __builtin_fmaf(zi, I_HI, -zh));
      prm[2 * tid] = (f32x4){zr * 1.4426950408889634f, zh, zl, 0.f};
      float cr = bf2f(Cre[d * N_ + tid]) * dt, ci = bf2f(Cim[d * N_ + tid]) * dt;
      prm[2 * tid + 1] = (f32x4){cr, ci, -cr, -ci};
    }
    __syncthreads();
    STAMP(0, 1);
    {
      const int i = lane & 15, kk = lane >> 4;
      const float pa0 = (float)((i & 7) + 128 * (i >> 3));
      const float pa1 = (float)(i < 8 ? 256 + 128 * (i >> 2) + 2 * (i & 3) : 512 + 128 * ((i - 8) >> 1) + 4 * (i & 1));
      const float pa2 = (float)(1024 + 128 * (i > 8 ? 8 : i));
      const float pb0 = (float)(8 * i);
      f32x4 acc[3];
#pragma unroll
      for (int sc = 0; sc < 3; ++sc) acc[sc] = (f32x4){0.f, 0.f, 0.f, 0.f};
      auto cexp = [&](const f32x4& z, float pp, float& er, float& ei) {
        float e = __builtin_amdgcn_exp2f(z[0] * pp);
        float r = __builtin_rintf(z[1] * pp);
        float f = __builtin_fmaf(z[1], pp, -r);
        f = __builtin_fmaf(z[2], pp, f);
        er = e * __builtin_amdgcn_cosf(f);
        asm("" : "+v"(er));
        ei = e * __builtin_amdgcn_sinf(f);
        asm("" : "+v"(ei));
      };
      auto split = [&](float re, float im, u32& hi, u32& lo) {
        hi = pack2(re, im);
        lo = pack2(re - lo16(hi), im - hi16(hi));
      };
      {
        u32x4 ah[3], al[3], bh, bl;
#pragma unroll
        for (int m = 0; m < 4; ++m) {
          const int n = 16 * wave + 4 * kk + m;
          f32x4 z = prm[2 * n], cc = prm[2 * n + 1];
          float er, ei;
          u32 h, l;
          cexp(z, pa0, er, ei); split(er, ei, h, l); ah[0][m] = h; al[0][m] = l;
          cexp(z, pa1, er, ei); split(er, ei, h, l); ah[1][m] = h; al[1][m] = l;
          cexp(z, pa2, er, ei); split(er, ei, h, l); ah[2][m] = h; al[2][m] = l;
          cexp(z, pb0, er, ei);
          float t0 = cc[1] * ei;
          asm("" : "+v"(t0));
          float bre = __builtin_fmaf(cc[0], er, -t0);
          float t1 = cc[2] * ei;
          asm("" : "+v"(t1));
          float bim = __builtin_fmaf(cc[3], er, t1);
          split(bre, bim, h, l); bh[m] = h; bl[m] = l;
        }
#pragma unroll
        for (int sc = 0; sc < 3; ++sc) {
          acc[sc] = __builtin_amdgcn_mfma_f32_16x16x32_bf16(__builtin_bit_cast(bf16x8, al[sc]), __builtin_bit_cast(bf16x8, bh), acc[sc], 0, 0, 0);
          acc[sc] = __builtin_amdgcn_mfma_f32_16x16x32_bf16(__builtin_bit_cast(bf16x8, ah[sc]), __builtin_bit_cast(bf16x8, bl), acc[sc], 0, 0, 0);
          acc[sc] = __builtin_amdgcn_mfma_f32_16x16x32_bf16(__builtin_bit_cast(bf16x8, ah[sc]), __builtin_bit_cast(bf16x8, bh), acc[sc], 0, 0, 0);
        }
      }
      float* tw = tab + wave * 768;
#pragma unroll
      for (int r = 0; r < 4; ++r) {
        int row = 4 * kk + r;
#pragma unroll
        for (int sc = 0; sc < 3; ++sc) tw[sc * 256 + 16 * row + i] = acc[sc][r];
      }
    }
    __syncthreads();
    for (int e = tid; e < 768; e += 512) {
      float v = ((tab[e] + tab[768 + e]) + (tab[2 * 768 + e] + tab[3 * 768 + e])) +
                ((tab[4 * 768 + e] + tab[5 * 768 + e]) + (tab[6 * 768 + e] + tab[7 * 768 + e]));
      tab[e] = 2.0f * v;
    }
    __syncthreads();
    STAMP(0, 2);
    {
      u16* kc = (u16*)(ws + WS_KC) + (size_t)d * 2 * L_;
      u32x2 hi, lo;
#pragma unroll
      for (int e2 = 0; e2 < 2; ++e2) {
        float kv[2];
#pragma unroll
        for (int e1 = 0; e1 < 2; ++e1) {
          int o = tid * 4 + 2 * e2 + e1;
          int l = (o & ~63) + 63 - (o & 63);
          int p = (int)rbf((float)l);
          int j = (p & 127) >> 3;
          int row;
          if (p < 256) row = (p & 7) + 8 * (p >> 7);
          else if (p < 512) row = 16 + (((p - 256) >> 7) << 2) + ((p & 7) >> 1);
          else if (p < 1024) row = 24 + (((p - 512) >> 7) << 1) + ((p & 7) >> 2);
          else row = 32 + ((p - 1024) >> 7);
          kv[e1] = tab[row * 16 + j];
        }
        hi[e2] = pack2(kv[0], kv[1]);
        lo[e2] = pack2(kv[0] - lo16(hi[e2]), kv[1] - hi16(hi[e2]));
      }
      GST(u32x2, kc + tid * 4, hi);
      GST(u32x2, kc + L_ + tid * 4, lo);
    }
    STAMP(0, 3);
    return;
  }
  b -= K1_NB_KGEN;
  __builtin_amdgcn_s_setprio(3);
  if (b < K1_NB_LN) {
    constexpr int LDW = 520;
    u16* sn = (u16*)smem;
    const int row0 = b * 16;
    u32x4 v[2];
#pragma unroll
    for (int r = 0; r < 2; ++r) v[r] = *(const u32x4*)(x + (size_t)(row0 + wave * 2 + r) * D_ + lane * 8);
#pragma unroll
    for (int r = 0; r < 2; ++r) {
      float f[8];
#pragma unroll
      for (int e = 0; e < 4; ++e) { f[2 * e] = lo16(v[r][e]); f[2 * e + 1] = hi16(v[r][e]); }
      float s = 0.f;
#pragma unroll
      for (int e = 0; e < 8; ++e) s += f[e];
      s = wave_sum(s);
      float mean = s * (1.0f / D_);
      float q = 0.f;
#pragma unroll
      for (int e = 0; e < 8; ++e) { float dd = f[e] - mean; q += dd * dd; }
      q = wave_sum(q);
      float var = q * (1.0f / D_);
      float mb = rbf(mean);
      float vb = rbf(var);
      float den = rbf(sqrtf(rbf(vb + 1e-5f)));
      float rinv = 1.0f / den;
      u32x4 ow;
#pragma unroll
      for (int e = 0; e < 4; ++e) {
        u32 dp = pack2(f[2 * e] - mb, f[2 * e + 1] - mb);
        ow[e] = pack2(lo16(dp) * rinv, hi16(dp) * rinv);
      }
      int lr = wave * 2 + r;
      *(u32x4*)(sn + lr * LDW + lane * 8) = ow;
    }
    STAMP(0, 1);
    __syncthreads();
    STAMP(0, 2);
    u16* xnf = (u16*)(ws + WS_XNF) + (size_t)b * 16 * 512;
#pragma unroll
    for (int it = 0; it < 2; ++it) {
      int pc = it * 512 + tid;
      int ln = pc & 63, ks = pc >> 6;
      int c = ln & 15, q = ln >> 4;
      u32x4 val = *(const u32x4*)(sn + c * LDW + ks * 32 + q * 8);
      GST(u32x4, xnf + (size_t)pc * 8, val);
    }
    {
      u16* xnt = (u16*)(ws + WS_XNT);
      const int cp = tid >> 1, hh = tid & 1;
      u32 w[8];
#pragma unroll
      for (int r = 0; r < 8; ++r) w[r] = *(const u32*)(sn + (8 * hh + r) * LDW + 2 * cp);
      u32x4 lo0, hi0;
#pragma unroll
      for (int e = 0; e < 4; ++e) {
        lo0[e] = __builtin_amdgcn_perm(w[2 * e + 1], w[2 * e], 0x05040100);
        hi0[e] = __builtin_amdgcn_perm(w[2 * e + 1], w[2 * e], 0x07060302);
      }
      u16* d0 = xnt + (size_t)(2 * cp) * L_ + row0 + 8 * hh;
      GST(u32x4, d0, lo0);
      GST(u32x4, d0 + L_, hi0);
    }
    STAMP(0, 3);
    return;
  }
  b -= K1_NB_LN;
  {
    constexpr int LDW = 136;
    u16* st = (u16*)smem;
    const u16* src;
    int ld, ks0, nch;
    int kind;
    if (b < K1_NB_W1) {
      kind = b / 56;
      int r = b % 56;
      ks0 = (r / 14) * 4; nch = r % 14;
      src = kind == 0 ? wg : wu;
      ld = F_;
    } else {
      int r = b - K1_NB_W1;
      kind = 2;
      ks0 = (r >> 2) * 4; nch = r & 3;
      src = wd;
      ld = D_;
    }
    u32x4 val[4];
#pragma unroll
    for (int h = 0; h < 4; ++h) {
      int r = (tid >> 4) + 32 * h, cc = (tid & 15) * 8;
      val[h] = *(const u32x4*)(src + (size_t)(ks0 * 32 + r) * ld + nch * 128 + cc);
    }
#pragma unroll
    for (int h = 0; h < 4; ++h) {
      int r = (tid >> 4) + 32 * h, cc = (tid & 15) * 8;
      *(u32x4*)(st + r * LDW + cc) = val[h];
    }
    __syncthreads();
#pragma unroll
    for (int h = 0; h < 4; ++h) {
      int ks = ks0 + h;
      int ln = tid & 63, nt = tid >> 6;
      int a = ln & 15, q = ln >> 4;
      const u16* sr = st + (h * 32 + 8 * q) * LDW + nt * 16 + a;
      u32x4 w;
#pragma unroll
      for (int e = 0; e < 4; ++e) w[e] = (u32)sr[(2 * e) * LDW] | ((u32)sr[(2 * e + 1) * LDW] << 16);
      int ft = nch * 8 + nt;
      u16* dst;
      if (kind < 2) {
        int fg = ft / 7, tau = ft % 7 + 7 * kind;
        dst = (u16*)(ws + WS_W1F) + ((size_t)(fg * 16 + ks) * 14 + tau) * 512 + ln * 8;
      } else {
        dst = (u16*)(ws + WS_W2F) + ((size_t)ft * 56 + ks) * 512 + ln * 8;
      }
      GST(u32x4, dst, w);
    }
  }
}

constexpr int CV_UCOPY = 4416;
constexpr int CV_KROW = 160;
constexpr int CV_KARR = 6400;
constexpr int CV_CH = 4 * CV_UCOPY + 2 * CV_KARR;
constexpr int G1_STAGE = 22 * 1024;
constexpr int K2_LDS = 3 * G1_STAGE;

#define MF(acc, A, B) acc = __builtin_amdgcn_mfma_f32_16x16x32_bf16(A, B, acc, 0, 0, 0)
template <int OFF> __device__ __forceinline__ u32x2 dsr64(u32 addr) {
  u32x2 r;
  asm volatile("ds_read_b64 %0, %1 offset:%2" : "=v"(r) : "v"(addr), "i"(OFF));
  return r;
}
template <int OFF> __device__ __forceinline__ bf16x8 dsr128(u32 addr) {
  bf16x8 r;
  asm volatile("ds_read_b128 %0, %1 offset:%2" : "=v"(r) : "v"(addr), "i"(OFF));
  return r;
}
template <int M> __device__ __forceinline__ bf16x8 ldW(u32 aaddr) {
  u32x2 x0 = dsr64<M * 32>(aaddr);
  u32x2 x1 = dsr64<M * 32 + 8>(aaddr);
  u32x4 v = {x0[0], x0[1], x1[0], x1[1]};
  return __builtin_bit_cast(bf16x8, v);
}
template <int N> __device__ __forceinline__ void wait_lgkm() {
  asm volatile("s_waitcnt lgkmcnt(%0)" ::"i"(N) : "memory");
  __builtin_amdgcn_sched_barrier(0);
}
struct ConvStep { bf16x8 w2, w3, w4, w5, h0, h1, l0, l1; };
template <int DL, int NDL> __device__ __forceinline__ void conv_load(ConvStep& st, u32 aaddr, u32 haddr, u32 laddr) {
  st.w2 = ldW<4 * DL + 2>(aaddr); st.w3 = ldW<4 * DL + 3>(aaddr);
  st.w4 = ldW<4 * DL + 4>(aaddr); st.w5 = ldW<4 * DL + 5>(aaddr);
  st.h0 = dsr128<(31 - DL) * CV_KROW>(haddr); st.h1 = dsr128<(31 - DL) * CV_KROW + 64>(haddr);
  if constexpr (DL < NDL) {
    st.l0 = dsr128<(31 - DL) * CV_KROW>(laddr); st.l1 = dsr128<(31 - DL) * CV_KROW + 64>(laddr);
  }
}
#define SB __builtin_amdgcn_sched_barrier(0)
template <int DL, int NDH, int NDL>
__device__ __forceinline__ void conv_step(ConvStep (&st)[3], bf16x8& w0, bf16x8& w1, u32 aaddr, u32 haddr, u32 laddr,
                                          f32x4 (&accH)[4], f32x4 (&accL)[4]) {
  if constexpr (DL < NDH) {
    constexpr int CN1 = (DL + 1 < NDH) ? ((DL + 1 < NDL) ? 12 : 10) : 0;
    constexpr bool PF = (DL + 2 < NDH);
    constexpr int DN = DL + 2;
    wait_lgkm<CN1>();
    ConvStep& c = st[DL % 3];
    ConvStep& n = st[(DL + 2) % 3];
    if constexpr (DL < NDL) {
      MF(accH[0], w0, c.h0); SB; if constexpr (PF) n.w2 = ldW<4 * DN + 2>(aaddr); SB;
      MF(accH[1], w1, c.h0); MF(accH[2], c.w2, c.h0); SB; if constexpr (PF) n.w3 = ldW<4 * DN + 3>(aaddr); SB;
      MF(accH[3], c.w3, c.h0); MF(accL[0], w0, c.l0); SB; if constexpr (PF) n.w4 = ldW<4 * DN + 4>(aaddr); SB;
      MF(accL[1], w1, c.l0); MF(accL[2], c.w2, c.l0); SB; if constexpr (PF) n.w5 = ldW<4 * DN + 5>(aaddr); SB;
      MF(accL[3], c.w3, c.l0); MF(accH[0], c.w2, c.h1); SB; if constexpr (PF) n.h0 = dsr128<(31 - DN) * CV_KROW>(haddr); SB;
      MF(accH[1], c.w3, c.h1); MF(accH[2], c.w4, c.h1); SB; if constexpr (PF) n.h1 = dsr128<(31 - DN) * CV_KROW + 64>(haddr); SB;
      MF(accH[3], c.w5, c.h1); MF(accL[0], c.w2, c.l1); SB; if constexpr (PF && DN < NDL) n.l0 = dsr128<(31 - DN) * CV_KROW>(laddr); SB;
      MF(accL[1], c.w3, c.l1); MF(accL[2], c.w4, c.l1); SB; if constexpr (PF && DN < NDL) n.l1 = dsr128<(31 - DN) * CV_KROW + 64>(laddr); SB;
      MF(accL[3], c.w5, c.l1); SB;
    } else {
      MF(accH[0], w0, c.h0); SB; if constexpr (PF) n.w2 = ldW<4 * DN + 2>(aaddr); SB;
      MF(accH[1], w1, c.h0); SB; if constexpr (PF) n.w3 = ldW<4 * DN + 3>(aaddr); SB;
      MF(accH[2], c.w2, c.h0); SB; if constexpr (PF) n.w4 = ldW<4 * DN + 4>(aaddr); SB;
      MF(accH[3], c.w3, c.h0); SB; if constexpr (PF) n.w5 = ldW<4 * DN + 5>(aaddr); SB;
      MF(accH[0], c.w2, c.h1); SB; if constexpr (PF) n.h0 = dsr128<(31 - DN) * CV_KROW>(haddr); SB;
      MF(accH[1], c.w3, c.h1); SB; if constexpr (PF) n.h1 = dsr128<(31 - DN) * CV_KROW + 64>(haddr); SB;
      MF(accH[2], c.w4, c.h1); MF(accH[3], c.w5, c.h1); SB;
    }
    w0 = c.w4; w1 = c.w5;
    conv_step<DL + 1, NDH, NDL>(st, w0, w1, aaddr, haddr, laddr, accH, accL);
  }
}
template <int NDH, int NDL>
__device__ __forceinline__ void conv_main(u32 aaddr, u32 haddr, u32 laddr, f32x4 (&accH)[4], f32x4 (&accL)[4]) {
  ConvStep st[3];
  bf16x8 w0 = ldW<0>(aaddr), w1 = ldW<1>(aaddr);
  conv_load<0, NDL>(st[0], aaddr, haddr, laddr);
  conv_load<1, NDL>(st[1], aaddr, haddr, laddr);
  conv_step<0, NDH, NDL>(st, w0, w1, aaddr, haddr, laddr, accH, accL);
}

__device__ __forceinline__ float gelu_tanh(float g) {
  const float c0 = 2.0f * 0.7978845608028654f * 1.4426950408889634f;
  const float c1 = c0 * 0.044715f;
  float g2 = g * g;
  float z = g * __builtin_fmaf(g2, c1, c0);
  float e = __builtin_amdgcn_exp2f(z);
  float r = __builtin_amdgcn_rcpf(1.0f + e);
  return __builtin_fmaf(-g, r, g);
}

__global__ __launch_bounds__(256, 2) void k2_kernel(const u16* __restrict__ dskip, char* __restrict__ ws) {
  __shared__ __attribute__((aligned(16))) char smem[K2_LDS];
  const int tid = threadIdx.x;
  const int lane = tid & 63;
  const int wave = __builtin_amdgcn_readfirstlane(tid >> 6);
  const int b = blockIdx.x;
  STAMP(1, 0);

  if (b < 256) {
    const int tt = ((b & 7) >> 2) * 8 + (b >> 5);
    const int fg = (b & 3) * 4 + ((b >> 3) & 3);
    const u16* w1 = (const u16*)(ws + WS_W1F) + (size_t)fg * 16 * 14 * 512;
    const u16* xf = (const u16*)(ws + WS_XNF) + (size_t)(tt * 8) * 16 * 512;
    f32x4 acc[14][2];
#pragma unroll
    for (int i = 0; i < 14; ++i) { acc[i][0] = (f32x4){0.f, 0.f, 0.f, 0.f}; acc[i][1] = (f32x4){0.f, 0.f, 0.f, 0.f}; }
    const u16* gsrc[6];
    int gdst[6];
    int gstep[6];
#pragma unroll
    for (int i = 0; i < 6; ++i) {
      int p = wave + 4 * i;
      if (p >= 22) p -= 22;
      if (p < 14) { gsrc[i] = w1 + p * 512 + lane * 8; gstep[i] = 14 * 512; }
      else { gsrc[i] = xf + (p - 14) * 16 * 512 + lane * 8; gstep[i] = 512; }
      gdst[i] = p * 1024 + lane * 16;
    }
    const int rot = 0;
    auto issue1 = [&](int ks, int i) {
      char* sb = smem + (ks % 3) * G1_STAGE;
      __builtin_amdgcn_global_load_lds((const __attribute__((address_space(1))) u32*)(gsrc[i] + ((ks + rot) & 15) * gstep[i]),
                                       (__attribute__((address_space(3))) u32*)(sb + gdst[i]), 16, 0, 0);
    };
#pragma unroll
    for (int i = 0; i < 6; ++i) issue1(0, i);
#pragma unroll
    for (int i = 0; i < 6; ++i) issue1(1, i);
#pragma unroll
    for (int i = 0; i < 6; ++i) issue1(2, i);
    asm volatile("s_waitcnt vmcnt(12)" ::: "memory");
    __builtin_amdgcn_s_barrier();
    bf16x8 alo[7], ahi[7], t0, t1;
    {
      const char* buf = smem + lane * 16;
      t0 = *(const bf16x8*)(buf + (14 + 2 * wave) * 1024);
      t1 = *(const bf16x8*)(buf + (15 + 2 * wave) * 1024);
#pragma unroll
      for (int t = 0; t < 7; ++t) alo[t] = *(const bf16x8*)(buf + t * 1024);
    }
#pragma unroll
    for (int ks = 0; ks < 16; ++ks) {
      const char* buf = smem + (ks % 3) * G1_STAGE + lane * 16;
#pragma unroll
      for (int t = 0; t < 7; ++t) {
        acc[t][0] = __builtin_amdgcn_mfma_f32_16x16x32_bf16(alo[t], t0, acc[t][0], 0, 0, 0);
        acc[t][1] = __builtin_amdgcn_mfma_f32_16x16x32_bf16(alo[t], t1, acc[t][1], 0, 0, 0);
        __builtin_amdgcn_sched_barrier(0);
        ahi[t] = *(const bf16x8*)(buf + (7 + t) * 1024);
        __builtin_amdgcn_sched_barrier(0);
      }
      bf16x8 n0 = t0, n1 = t1;
      if (ks < 15) {
        if (ks < 14) asm volatile("s_waitcnt vmcnt(6) lgkmcnt(0)" ::: "memory");
        else asm volatile("s_waitcnt vmcnt(0) lgkmcnt(0)" ::: "memory");
        __builtin_amdgcn_s_barrier();
        __builtin_amdgcn_sched_barrier(0);
      }
      const char* nb = smem + ((ks + 1) % 3) * G1_STAGE + lane * 16;
#pragma unroll
      for (int t = 0; t < 7; ++t) {
        acc[7 + t][0] = __builtin_amdgcn_mfma_f32_16x16x32_bf16(ahi[t], t0, acc[7 + t][0], 0, 0, 0);
        __builtin_amdgcn_sched_barrier(0);
        if (ks < 15) {
          alo[t] = *(const bf16x8*)(nb + t * 1024);
          if (t == 0) n0 = *(const bf16x8*)(nb + (14 + 2 * wave) * 1024);
          if (t == 1) n1 = *(const bf16x8*)(nb + (15 + 2 * wave) * 1024);
          __builtin_amdgcn_sched_barrier(0);
        }
        acc[7 + t][1] = __builtin_amdgcn_mfma_f32_16x16x32_bf16(ahi[t], t1, acc[7 + t][1], 0, 0, 0);
        if (t < 6 && ks + 3 < 16) {
          __builtin_amdgcn_sched_barrier(0);
          issue1(ks + 3, t);
        }
        __builtin_amdgcn_sched_barrier(0);
      }
      t0 = n0; t1 = n1;
    }
    STAMP(1, 2);
    u16* hf = (u16*)(ws + WS_HF);
    const int c = lane & 15, Q = lane >> 4;
#pragma unroll
    for (int t = 0; t < 7; ++t) {
      int ft = fg * 7 + t;
      int ks2 = ft >> 1;
      int q = 2 * (ft & 1) + (Q >> 1);
#pragma unroll
      for (int m = 0; m < 2; ++m) {
        float hv[4];
#pragma unroll
        for (int r = 0; r < 4; ++r) {
          float g = rbf(acc[t][m][r]);
          float u = rbf(acc[t + 7][m][r]);
          hv[r] = rbf(gelu_tanh(g)) * u;
        }
        int tile = tt * 8 + 2 * wave + m;
        u32x2 o = {pack2(hv[0], hv[1]), pack2(hv[2], hv[3])};
        GST2(u32x2, hf + (((size_t)tile * 56 + ks2) * 64 + (c + 16 * q)) * 8 + 4 * (Q & 1), o);
      }
    }
    STAMP(1, 3);
    return;
  }

  {
    const int chl = wave >> 1;
    const int d = (b - 256) * 2 + chl;
    const int t128 = tid & 127;
    char* cb = smem + chl * CV_CH;
    const u16* ut = (const u16*)(ws + WS_XNT) + (size_t)d * L_;
    const u16* kc = (const u16*)(ws + WS_KC) + (size_t)d * 2 * L_;
    char* kb = cb + 4 * CV_UCOPY;
    {
      u32x4 v0[3], v1[3], kv[4];
#pragma unroll
      for (int it = 0; it < 3; ++it) {
        int cch = it * 128 + t128 - 8;
        int c0 = cch < 0 ? 0 : (cch > 255 ? 255 : cch);
        int c1 = cch + 1 < 0 ? 0 : (cch + 1 > 255 ? 255 : cch + 1);
        v0[it] = *(const u32x4*)(ut + c0 * 8);
        v1[it] = *(const u32x4*)(ut + c1 * 8);
      }
#pragma unroll
      for (int it = 0; it < 4; ++it) {
        int pc = it * 128 + t128;
        kv[it] = *(const u32x4*)(kc + pc * 8);
      }
      __builtin_amdgcn_sched_barrier(0);
#pragma unroll
      for (int it = 0; it < 3; ++it) {
        int X = it * 128 + t128;
        int cch = X - 8;
        bool ok0 = (cch >= 0 && cch < 256), ok1 = (cch + 1 >= 0 && cch + 1 < 256);
        u32 w[6] = {ok0 ? v0[it][0] : 0u, ok0 ? v0[it][1] : 0u, ok0 ? v0[it][2] : 0u, ok0 ? v0[it][3] : 0u,
                    ok1 ? v1[it][0] : 0u, ok1 ? v1[it][1] : 0u};
        u32 s1[5];
#pragma unroll
        for (int e = 0; e < 5; ++e) s1[e] = __builtin_amdgcn_alignbit(w[e + 1], w[e], 16);
        if (X < 276) {
          *(u32x4*)(cb + 0 * CV_UCOPY + X * 16) = (u32x4){w[0], w[1], w[2], w[3]};
          *(u32x4*)(cb + 1 * CV_UCOPY + X * 16) = (u32x4){s1[0], s1[1], s1[2], s1[3]};
          *(u32x4*)(cb + 2 * CV_UCOPY + X * 16) = (u32x4){w[1], w[2], w[3], w[4]};
          *(u32x4*)(cb + 3 * CV_UCOPY + X * 16) = (u32x4){s1[1], s1[2], s1[3], s1[4]};
        }
      }
#pragma unroll
      for (int it = 0; it < 4; ++it) {
        int pc = it * 128 + t128;
        int arr = pc >> 8, row = (pc >> 3) & 31, ch = pc & 7;
        *(u32x4*)(kb + arr * CV_KARR + (row + 7) * CV_KROW + ch * 16) = kv[it];
      }
    }
    if (t128 < 112) {
      int arr = t128 / 56, r = t128 % 56;
      *(uint4*)(kb + arr * CV_KARR + (r >> 3) * CV_KROW + (r & 7) * 16) = make_uint4(0, 0, 0, 0);
    }
    __syncthreads();
    STAMP(1, 1);

    const int p = wave & 1;
    const int gH = p ? 2 : 3, gL = p ? 1 : 0;
    const int a = lane & 15, q = lane >> 4;
    const char* abase = cb + ((a + 1) & 3) * CV_UCOPY + (8 * q + 4 * ((a + 1) >> 2)) * 2;
    const char* bH = kb + (a >= 8 ? CV_KARR : 0) + (8 * gH + (a & 7) + 7) * CV_KROW + 16 * q;
    const char* bL = kb + (a >= 8 ? CV_KARR : 0) + (8 * gL + (a & 7) + 7) * CV_KROW + 16 * q;
    f32x4 accH[4], accL[4];
#pragma unroll
    for (int i = 0; i < 4; ++i) { accH[i] = (f32x4){0.f, 0.f, 0.f, 0.f}; accL[i] = (f32x4){0.f, 0.f, 0.f, 0.f}; }
    const int col = (a < 8) ? (8 * gH + a) : (8 * gL + a - 8);
    u32x2 uvp[4];
#pragma unroll
    for (int rt = 0; rt < 4; ++rt) {
      int t0 = 64 * col + 16 * rt + 4 * q;
      uvp[rt] = *(const u32x2*)(ut + t0);
    }
    const float dk = bf2f(dskip[d]);
    __builtin_amdgcn_sched_barrier(0);
    {
      typedef __attribute__((address_space(3))) const char* lptr;
      u32 aaddr = (u32)(size_t)(lptr)abase;
      u32 haddr = (u32)(size_t)(lptr)bH - 31 * CV_KROW;
      u32 laddr = (u32)(size_t)(lptr)bL - 31 * CV_KROW;
      if (p) conv_main<24, 16>(aaddr, haddr, laddr, accH, accL);
      else conv_main<32, 8>(aaddr, haddr, laddr, accH, accL);
    }
    STAMP(1, 2);
    u16* r1 = (u16*)(ws + WS_R1T) + (size_t)d * L_;
#pragma unroll
    for (int rt = 0; rt < 4; ++rt) {
      int t0 = 64 * col + 16 * rt + 4 * q;
      float o[4];
#pragma unroll
      for (int r = 0; r < 4; ++r) {
        float mine = (a < 8) ? accH[rt][r] : accL[rt][r];
        float give = (a < 8) ? accL[rt][r] : accH[rt][r];
        float other = __int_as_float(__builtin_amdgcn_update_dpp(0, __float_as_int(give), 0x128, 0xf, 0xf, true));
        float y = rbf(mine + other);
        float un = bf2f((u16)(uvp[rt][r >> 1] >> (16 * (r & 1))));
        o[r] = y + rbf(un * dk);
      }
      GST2(u32x2, r1 + t0, ((u32x2){pack2(o[0], o[1]), pack2(o[2], o[3])}));
    }
    STAMP(1, 3);
  }
}

__global__ __launch_bounds__(256) void k3_kernel(const u16* __restrict__ x, char* __restrict__ ws, u16* __restrict__ out) {
  __shared__ __attribute__((aligned(16))) f32x4 red[4][16][64];
  const int tid = threadIdx.x;
  const int lane = tid & 63;
  const int wave = __builtin_amdgcn_readfirstlane(tid >> 6);
  const int b = blockIdx.x;
  STAMP(2, 0);
  const int tb = (b & 7) * 4 + ((b >> 3) & 3);
  const int db = b >> 5;
  const u16* ha = (const u16*)(ws + WS_HF) + ((size_t)(tb * 4) * 56 + wave * 14) * 512 + lane * 8;
  const u16* wb = (const u16*)(ws + WS_W2F) + ((size_t)(db * 4) * 56 + wave * 14) * 512 + lane * 8;
  f32x4 acc[4][4];
#pragma unroll
  for (int m = 0; m < 4; ++m)
#pragma unroll
    for (int n = 0; n < 4; ++n) acc[m][n] = (f32x4){0.f, 0.f, 0.f, 0.f};
  constexpr int PF = 4;
  bf16x8 A[PF][4], B[PF][4];
#pragma unroll
  for (int s = 0; s < PF; ++s)
#pragma unroll
    for (int m = 0; m < 4; ++m) {
      A[s][m] = *(const bf16x8*)(ha + ((size_t)m * 56 + s) * 512);
      B[s][m] = *(const bf16x8*)(wb + ((size_t)m * 56 + s) * 512);
    }
  __builtin_amdgcn_sched_barrier(0);
#pragma unroll
  for (int s = 0; s < 14; ++s) {
    const int sl = s % PF;
#pragma unroll
    for (int m = 0; m < 4; ++m)
#pragma unroll
      for (int n = 0; n < 4; ++n)
        acc[m][n] = __builtin_amdgcn_mfma_f32_16x16x32_bf16(A[sl][m], B[sl][n], acc[m][n], 0, 0, 0);
    __builtin_amdgcn_sched_barrier(0);
    if (s + PF < 14) {
#pragma unroll
      for (int m = 0; m < 4; ++m) {
        A[sl][m] = *(const bf16x8*)(ha + ((size_t)m * 56 + s + PF) * 512);
        B[sl][m] = *(const bf16x8*)(wb + ((size_t)m * 56 + s + PF) * 512);
      }
    }
    __builtin_amdgcn_sched_barrier(0);
  }
  STAMP(2, 1);
  const int c = lane & 15, Q = lane >> 4;
  const u16* at = (const u16*)(ws + WS_R1T);
  const int tok0 = tb * 64 + wave * 16 + 4 * Q;
  u32x2 av[4];
#pragma unroll
  for (int n = 0; n < 4; ++n) av[n] = *(const u32x2*)(at + (size_t)(db * 64 + n * 16 + c) * L_ + tok0);
  const int orow = tb * 64 + wave * 16 + (lane >> 2);
  const int ocol = db * 64 + (lane & 3) * 16;
  u32x4 xv0 = *(const u32x4*)(x + (size_t)orow * D_ + ocol);
  u32x4 xv1 = *(const u32x4*)(x + (size_t)orow * D_ + ocol + 8);
#pragma unroll
  for (int m = 0; m < 4; ++m)
#pragma unroll
    for (int n = 0; n < 4; ++n) red[wave][m * 4 + n][lane] = acc[m][n];
  __syncthreads();
  STAMP(2, 2);
  float dn[4][4];
#pragma unroll
  for (int n = 0; n < 4; ++n) {
    f32x4 v = red[0][wave * 4 + n][lane];
#pragma unroll
    for (int w = 1; w < 4; ++w) {
      f32x4 t = red[w][wave * 4 + n][lane];
      v[0] += t[0]; v[1] += t[1]; v[2] += t[2]; v[3] += t[3];
    }
#pragma unroll
    for (int r = 0; r < 4; ++r) dn[n][r] = v[r];
  }
  __syncthreads();
  u32* tp = (u32*)red + wave * (16 * 68);
#pragma unroll
  for (int n = 0; n < 4; ++n)
#pragma unroll
    for (int r = 0; r < 4; ++r) {
      u32 a16 = (r & 1) ? (av[n][r >> 1] >> 16) : (av[n][r >> 1] & 0xffffu);
      tp[(4 * Q + r) * 68 + n * 16 + c] = a16 | ((u32)f2bf(dn[n][r]) << 16);
    }
  __builtin_amdgcn_s_waitcnt(0xC07F);
  __builtin_amdgcn_wave_barrier();
  {
    const u32* rp = tp + (lane >> 2) * 68 + (lane & 3) * 16;
    u32x4 o[2];
#pragma unroll
    for (int h = 0; h < 2; ++h) {
      u32x4 p0 = *(const u32x4*)(rp + h * 8), p1 = *(const u32x4*)(rp + h * 8 + 4);
      u32x4 xv = h ? xv1 : xv0;
#pragma unroll
      for (int e = 0; e < 4; ++e) {
        u32 q0 = e < 2 ? p0[2 * e] : p1[2 * e - 4], q1 = e < 2 ? p0[2 * e + 1] : p1[2 * e - 3];
        u32 r1 = pack2(lo16(xv[e]) + lo16(q0), hi16(xv[e]) + lo16(q1));
        o[h][e] = pack2(lo16(r1) + hi16(q0), hi16(r1) + hi16(q1));
      }
    }
    GST(u32x4, out + (size_t)orow * D_ + ocol, o[0]);
    GST(u32x4, out + (size_t)orow * D_ + ocol + 8, o[1]);
  }
  STAMP(2, 3);
}

extern "C" void kernel_launch(void* const* d_in, const int* in_sizes, int n_in, void* d_out, int out_size,
                              void* d_ws, size_t ws_size, hipStream_t stream) {
  const u16* x = (const u16*)d_in[0];
  const u16* logA = (const u16*)d_in[1];
  const u16* Aim = (const u16*)d_in[2];
  const u16* Cre = (const u16*)d_in[3];
  const u16* Cim = (const u16*)d_in[4];
  const u16* logdt = (const u16*)d_in[5];
  const u16* dskip = (const u16*)d_in[6];
  const u16* wg = (const u16*)d_in[8];
  const u16* wu = (const u16*)d_in[9];
  const u16* wd = (const u16*)d_in[10];
  char* ws = (char*)d_ws;
  k1_kernel<<<K1_GRID, 512, 0, stream>>>(x, logA, Aim, Cre, Cim, logdt, wg, wu, wd, ws);
  k2_kernel<<<512, 256, 0, stream>>>(dskip, ws);
  k3_kernel<<<256, 256, 0, stream>>>(x, ws, (u16*)d_out);
}
```

```cpp
#include <hip/hip_runtime.h>
#include <hip/hip_bf16.h>

typedef __attribute__((ext_vector_type(8))) short bf16x8;
typedef __attribute__((ext_vector_type(4))) float f32x4;
typedef unsigned short u16;
typedef unsigned int u32;
typedef __attribute__((ext_vector_type(4))) unsigned int u32x4;
typedef __attribute__((ext_vector_type(2))) unsigned int u32x2;

constexpr int L_ = 2048;
constexpr int D_ = 512;
constexpr int N_ = 128;
constexpr int F_ = 1792;

constexpr size_t WS_XNF = 0;
constexpr size_t WS_XNT = WS_XNF + (size_t)L_ * D_ * 2;
constexpr size_t WS_XT = WS_XNT + (size_t)L_ * D_ * 2;
constexpr size_t WS_W1F = WS_XT + (size_t)L_ * D_ * 2;
constexpr size_t WS_W2F = WS_W1F + (size_t)D_ * F_ * 2 * 2;
constexpr size_t WS_KC = WS_W2F + (size_t)D_ * F_ * 2;
constexpr size_t WS_HF = WS_KC + (size_t)D_ * 2 * L_ * 2;
constexpr size_t WS_R1T = WS_HF + (size_t)L_ * F_ * 2;
constexpr size_t WS_END = WS_R1T + (size_t)D_ * L_ * 2;

#define STAMP(kid, idx)
template <typename T> __device__ __forceinline__ void gst_sc1(T* p, T v);
template <> __device__ __forceinline__ void gst_sc1<u32x4>(u32x4* p, u32x4 v) { asm volatile("global_store_dwordx4 %0, %1, off sc1\n\ts_nop 1" :: "v"(p), "v"(v) : "memory"); }
template <> __device__ __forceinline__ void gst_sc1<u32x2>(u32x2* p, u32x2 v) { asm volatile("global_store_dwordx2 %0, %1, off sc1\n\ts_nop 1" :: "v"(p), "v"(v) : "memory"); }
#define GST(T, p, v) gst_sc1<T>((T*)(p), (T)(v))
#define GST2 GST
__device__ __forceinline__ float bf2f(u16 v) { return __uint_as_float(((u32)v) << 16); }
__device__ __forceinline__ u16 f2bf(float f) {
  __bf16 b = (__bf16)f;
  return __builtin_bit_cast(u16, b);
}
__device__ __forceinline__ float rbf(float f) { return bf2f(f2bf(f)); }
__device__ __forceinline__ u32 pack2(float lo, float hi) {
  typedef __attribute__((ext_vector_type(2))) __bf16 bf2;
  bf2 v = {(__bf16)lo, (__bf16)hi};
  return __builtin_bit_cast(u32, v);
}
__device__ __forceinline__ float lo16(u32 w) { return __uint_as_float(w << 16); }
__device__ __forceinline__ float hi16(u32 w) { return __uint_as_float(w & 0xffff0000u); }

__device__ __forceinline__ float wave_sum(float v) {
  int x = __float_as_int(v);
  v += __int_as_float(__builtin_amdgcn_update_dpp(0, x, 0xB1, 0xf, 0xf, true));
  x = __float_as_int(v);
  v += __int_as_float(__builtin_amdgcn_update_dpp(0, x, 0x4E, 0xf, 0xf, true));
  x = __float_as_int(v);
  v += __int_as_float(__builtin_amdgcn_update_dpp(0, x, 0x141, 0xf, 0xf, true));
  x = __float_as_int(v);
  v += __int_as_float(__builtin_amdgcn_update_dpp(0, x, 0x140, 0xf, 0xf, true));
  x = __float_as_int(v);
  float a = __int_as_float(__builtin_amdgcn_readlane(x, 0));
  float b = __int_as_float(__builtin_amdgcn_readlane(x, 16));
  float c = __int_as_float(__builtin_amdgcn_readlane(x, 32));
  float d = __int_as_float(__builtin_amdgcn_readlane(x, 48));
  return (a + b) + (c + d);
}

constexpr int K1_NB_KGEN = 512;
constexpr int K1_NB_LN = 128;
constexpr int K1_NB_W1 = 112;
constexpr int K1_NB_W2 = 56;
constexpr int K1_GRID = K1_NB_KGEN + K1_NB_LN + K1_NB_W1 + K1_NB_W2;

__global__ __launch_bounds__(512) void k1_kernel(
    const u16* __restrict__ x, const u16* __restrict__ logA, const u16* __restrict__ Aim,
    const u16* __restrict__ Cre, const u16* __restrict__ Cim, const u16* __restrict__ logdt,
    const u16* __restrict__ wg, const u16* __restrict__ wu, const u16* __restrict__ wd,
    char* __restrict__ ws) {
  __shared__ __attribute__((aligned(16))) char smem[128 * 136 * 2];
  const int tid = threadIdx.x;
  const int lane = tid & 63;
  const int wave = tid >> 6;
  int b = blockIdx.x;
  STAMP(0, 0);

  if (b < K1_NB_KGEN) {
    const int d = b;
    f32x4* prm = (f32x4*)smem;
    float* tab = (float*)(smem + 4096);
    if (tid < N_) {
      float dt = rbf(expf(bf2f(logdt[d])));
      float ar = rbf(expf(bf2f(logA[d * N_ + tid])));
      float zr = -ar * dt;
      float zi = bf2f(Aim[d * N_ + tid]) * dt;
      const float I_HI = 0.15915494309189535f;
      const float I_LO = 6.4206382e-9f;
      float zh = zi * I_HI;
      float zl = __builtin_fmaf(zi, I_LO, __builtin_fmaf(zi, I_HI, -zh));
      prm[2 * tid] = (f32x4){zr * 1.4426950408889634f, zh, zl, 0.f};
      float cr = bf2f(Cre[d * N_ + tid]) * dt, ci = bf2f(Cim[d * N_ + tid]) * dt;
      prm[2 * tid + 1] = (f32x4){cr, ci, -cr, -ci};
    }
    __syncthreads();
    STAMP(0, 1);
    {
      const int i = lane & 15, kk = lane >> 4;
      const float pa0 = (float)((i & 7) + 128 * (i >> 3));
      const float pa1 = (float)(i < 8 ? 256 + 128 * (i >> 2) + 2 * (i & 3) : 512 + 128 * ((i - 8) >> 1) + 4 * (i & 1));
      const float pa2 = (float)(1024 + 128 * (i > 8 ? 8 : i));
      const float pb0 = (float)(8 * i);
      f32x4 acc[3];
#pragma unroll
      for (int sc = 0; sc < 3; ++sc) acc[sc] = (f32x4){0.f, 0.f, 0.f, 0.f};
      auto cexp = [&](const f32x4& z, float pp, float& er, float& ei) {
        float e = __builtin_amdgcn_exp2f(z[0] * pp);
        float r = __builtin_rintf(z[1] * pp);
        float f = __builtin_fmaf(z[1], pp, -r);
        f = __builtin_fmaf(z[2], pp, f);
        er = e * __builtin_amdgcn_cosf(f);
        asm("" : "+v"(er));
        ei = e * __builtin_amdgcn_sinf(f);
        asm("" : "+v"(ei));
      };
      auto split = [&](float re, float im, u32& hi, u32& lo) {
        hi = pack2(re, im);
        lo = pack2(re - lo16(hi), im - hi16(hi));
      };
      {
        u32x4 ah[3], al[3], bh, bl;
#pragma unroll
        for (int m = 0; m < 4; ++m) {
          const int n = 16 * wave + 4 * kk + m;
          f32x4 z = prm[2 * n], cc = prm[2 * n + 1];
          float er, ei;
          u32 h, l;
          cexp(z, pa0, er, ei); split(er, ei, h, l); ah[0][m] = h; al[0][m] = l;
          cexp(z, pa1, er, ei); split(er, ei, h, l); ah[1][m] = h; al[1][m] = l;
          cexp(z, pa2, er, ei); split(er, ei, h, l); ah[2][m] = h; al[2][m] = l;
          cexp(z, pb0, er, ei);
          float t0 = cc[1] * ei;
          asm("" : "+v"(t0));
          float bre = __builtin_fmaf(cc[0], er, -t0);
          float t1 = cc[2] * ei;
          asm("" : "+v"(t1));
          float bim = __builtin_fmaf(cc[3], er, t1);
          split(bre, bim, h, l); bh[m] = h; bl[m] = l;
        }
#pragma unroll
        for (int sc = 0; sc < 3; ++sc) {
          acc[sc] = __builtin_amdgcn_mfma_f32_16x16x32_bf16(__builtin_bit_cast(bf16x8, al[sc]), __builtin_bit_cast(bf16x8, bh), acc[sc], 0, 0, 0);
          acc[sc] = __builtin_amdgcn_mfma_f32_16x16x32_bf16(__builtin_bit_cast(bf16x8, ah[sc]), __builtin_bit_cast(bf16x8, bl), acc[sc], 0, 0, 0);
          acc[sc] = __builtin_amdgcn_mfma_f32_16x16x32_bf16(__builtin_bit_cast(bf16x8, ah[sc]), __builtin_bit_cast(bf16x8, bh), acc[sc], 0, 0, 0);
        }
      }
      float* tw = tab + wave * 768;
#pragma unroll
      for (int r = 0; r < 4; ++r) {
        int row = 4 * kk + r;
#pragma unroll
        for (int sc = 0; sc < 3; ++sc) tw[sc * 256 + 16 * row + i] = acc[sc][r];
      }
    }
    __syncthreads();
    for (int e = tid; e < 768; e += 512) {
      float v = ((tab[e] + tab[768 + e]) + (tab[2 * 768 + e] + tab[3 * 768 + e])) +
                ((tab[4 * 768 + e] + tab[5 * 768 + e]) + (tab[6 * 768 + e] + tab[7 * 768 + e]));
      tab[e] = 2.0f * v;
    }
    __syncthreads();
    STAMP(0, 2);
    {
      u16* kc = (u16*)(ws + WS_KC) + (size_t)d * 2 * L_;
      u32x2 hi, lo;
#pragma unroll
      for (int e2 = 0; e2 < 2; ++e2) {
        float kv[2];
#pragma unroll
        for (int e1 = 0; e1 < 2; ++e1) {
          int o = tid * 4 + 2 * e2 + e1;
          int l = (o & ~63) + 63 - (o & 63);
          int p = (int)rbf((float)l);
          int j = (p & 127) >> 3;
          int row;
          if (p < 256) row = (p & 7) + 8 * (p >> 7);
          else if (p < 512) row = 16 + (((p - 256) >> 7) << 2) + ((p & 7) >> 1);
          else if (p < 1024) row = 24 + (((p - 512) >> 7) << 1) + ((p & 7) >> 2);
          else row = 32 + ((p - 1024) >> 7);
          kv[e1] = tab[row * 16 + j];
        }
        hi[e2] = pack2(kv[0], kv[1]);
        lo[e2] = pack2(kv[0] - lo16(hi[e2]), kv[1] - hi16(hi[e2]));
      }
      GST(u32x2, kc + tid * 4, hi);
      GST(u32x2, kc + L_ + tid * 4, lo);
    }
    STAMP(0, 3);
    return;
  }
  b -= K1_NB_KGEN;
  __builtin_amdgcn_s_setprio(3);
  if (b < K1_NB_LN) {
    constexpr int LDW = 520;
    u16* sn = (u16*)smem;
    const int row0 = b * 16;
    u32x4 v[2];
#pragma unroll
    for (int r = 0; r < 2; ++r) v[r] = *(const u32x4*)(x + (size_t)(row0 + wave * 2 + r) * D_ + lane * 8);
#pragma unroll
    for (int r = 0; r < 2; ++r) {
      float f[8];
#pragma unroll
      for (int e = 0; e < 4; ++e) { f[2 * e] = lo16(v[r][e]); f[2 * e + 1] = hi16(v[r][e]); }
      float s = 0.f;
#pragma unroll
      for (int e = 0; e < 8; ++e) s += f[e];
      s = wave_sum(s);
      float mean = s * (1.0f / D_);
      float q = 0.f;
#pragma unroll
      for (int e = 0; e < 8; ++e) { float dd = f[e] - mean; q += dd * dd; }
      q = wave_sum(q);
      float var = q * (1.0f / D_);
      float mb = rbf(mean);
      float vb = rbf(var);
      float den = rbf(sqrtf(rbf(vb + 1e-5f)));
      float rinv = 1.0f / den;
      u32x4 ow;
#pragma unroll
      for (int e = 0; e < 4; ++e) {
        u32 dp = pack2(f[2 * e] - mb, f[2 * e + 1] - mb);
        ow[e] = pack2(lo16(dp) * rinv, hi16(dp) * rinv);
      }
      int lr = wave * 2 + r;
      *(u32x4*)(sn + lr * LDW + lane * 8) = ow;
    }
    STAMP(0, 1);
    __syncthreads();
    STAMP(0, 2);
    u16* xnf = (u16*)(ws + WS_XNF) + (size_t)b * 16 * 512;
#pragma unroll
    for (int it = 0; it < 2; ++it) {
      int pc = it * 512 + tid;
      int ln = pc & 63, ks = pc >> 6;
      int c = ln & 15, q = ln >> 4;
      u32x4 val = *(const u32x4*)(sn + c * LDW + ks * 32 + q * 8);
      GST(u32x4, xnf + (size_t)pc * 8, val);
    }
    {
      u16* xnt = (u16*)(ws + WS_XNT);
      const int cp = tid >> 1, hh = tid & 1;
      u32 w[8];
#pragma unroll
      for (int r = 0; r < 8; ++r) w[r] = *(const u32*)(sn + (8 * hh + r) * LDW + 2 * cp);
      u32x4 lo0, hi0;
#pragma unroll
      for (int e = 0; e < 4; ++e) {
        lo0[e] = __builtin_amdgcn_perm(w[2 * e + 1], w[2 * e], 0x05040100);
        hi0[e] = __builtin_amdgcn_perm(w[2 * e + 1], w[2 * e], 0x07060302);
      }
      u16* d0 = xnt + (size_t)(2 * cp) * L_ + row0 + 8 * hh;
      GST(u32x4, d0, lo0);
      GST(u32x4, d0 + L_, hi0);
    }
    STAMP(0, 3);
    return;
  }
  b -= K1_NB_LN;
  {
    constexpr int LDW = 136;
    u16* st = (u16*)smem;
    const u16* src;
    int ld, ks0, nch;
    int kind;
    if (b < K1_NB_W1) {
      kind = b / 56;
      int r = b % 56;
      ks0 = (r / 14) * 4; nch = r % 14;
      src = kind == 0 ? wg : wu;
      ld = F_;
    } else {
      int r = b - K1_NB_W1;
      kind = 2;
      ks0 = (r >> 2) * 4; nch = r & 3;
      src = wd;
      ld = D_;
    }
    u32x4 val[4];
#pragma unroll
    for (int h = 0; h < 4; ++h) {
      int r = (tid >> 4) + 32 * h, cc = (tid & 15) * 8;
      val[h] = *(const u32x4*)(src + (size_t)(ks0 * 32 + r) * ld + nch * 128 + cc);
    }
#pragma unroll
    for (int h = 0; h < 4; ++h) {
      int r = (tid >> 4) + 32 * h, cc = (tid & 15) * 8;
      *(u32x4*)(st + r * LDW + cc) = val[h];
    }
    __syncthreads();
#pragma unroll
    for (int h = 0; h < 4; ++h) {
      int ks = ks0 + h;
      int ln = tid & 63, nt = tid >> 6;
      int a = ln & 15, q = ln >> 4;
      const u16* sr = st + (h * 32 + 8 * q) * LDW + nt * 16 + a;
      u32x4 w;
#pragma unroll
      for (int e = 0; e < 4; ++e) w[e] = (u32)sr[(2 * e) * LDW] | ((u32)sr[(2 * e + 1) * LDW] << 16);
      int ft = nch * 8 + nt;
      u16* dst;
      if (kind < 2) {
        int fg = ft / 7, tau = ft % 7 + 7 * kind;
        dst = (u16*)(ws + WS_W1F) + ((size_t)(fg * 16 + ks) * 14 + tau) * 512 + ln * 8;
      } else {
        dst = (u16*)(ws + WS_W2F) + ((size_t)ft * 56 + ks) * 512 + ln * 8;
      }
      GST(u32x4, dst, w);
    }
  }
}

constexpr int CV_UCOPY = 4416;
constexpr int CV_KROW = 160;
constexpr int CV_KARR = 6400;
constexpr int CV_CH = 4 * CV_UCOPY + 2 * CV_KARR;
constexpr int G1_STAGE = 22 * 1024;
constexpr int K2_LDS = 3 * G1_STAGE;

#define MF(acc, A, B) acc = __builtin_amdgcn_mfma_f32_16x16x32_bf16(A, B, acc, 0, 0, 0)
template <int OFF> __device__ __forceinline__ u32x2 dsr64(u32 addr) {
  u32x2 r;
  asm volatile("ds_read_b64 %0, %1 offset:%2" : "=v"(r) : "v"(addr), "i"(OFF));
  return r;
}
template <int OFF> __device__ __forceinline__ bf16x8 dsr128(u32 addr) {
  bf16x8 r;
  asm volatile("ds_read_b128 %0, %1 offset:%2" : "=v"(r) : "v"(addr), "i"(OFF));
  return r;
}
template <int M> __device__ __forceinline__ bf16x8 ldW(u32 aaddr) {
  u32x2 x0 = dsr64<M * 32>(aaddr);
  u32x2 x1 = dsr64<M * 32 + 8>(aaddr);
  u32x4 v = {x0[0], x0[1], x1[0], x1[1]};
  return __builtin_bit_cast(bf16x8, v);
}
template <int N> __device__ __forceinline__ void wait_lgkm() {
  asm volatile("s_waitcnt lgkmcnt(%0)" ::"i"(N) : "memory");
  __builtin_amdgcn_sched_barrier(0);
}
struct ConvStep { bf16x8 w2, w3, w4, w5, h0, h1, l0, l1; };
#define SB __builtin_amdgcn_sched_barrier(0)
template <int J, bool BOTH>
__device__ __forceinline__ void conv_step(ConvStep (&st)[4], bf16x8& w0, bf16x8& w1, u32 aaddr, u32 haddr, u32 laddr,
                                          f32x4 (&accH)[4], f32x4 (&accL)[4]) {
  constexpr int DN = J + 2;
  wait_lgkm<BOTH ? 12 : 10>();
  ConvStep& c = st[J & 3];
  ConvStep& n = st[(J + 2) & 3];
  if constexpr (BOTH) {
    MF(accH[0], w0, c.h0); SB; n.w2 = ldW<4 * DN + 2>(aaddr); SB;
    MF(accH[1], w1, c.h0); MF(accH[2], c.w2, c.h0); SB; n.w3 = ldW<4 * DN + 3>(aaddr); SB;
    MF(accH[3], c.w3, c.h0); MF(accL[0], w0, c.l0); SB; n.w4 = ldW<4 * DN + 4>(aaddr); SB;
    MF(accL[1], w1, c.l0); MF(accL[2], c.w2, c.l0); SB; n.w5 = ldW<4 * DN + 5>(aaddr); SB;
    MF(accL[3], c.w3, c.l0); MF(accH[0], c.w2, c.h1); SB; n.h0 = dsr128<(5 - DN) * CV_KROW>(haddr); SB;
    MF(accH[1], c.w3, c.h1); MF(accH[2], c.w4, c.h1); SB; n.h1 = dsr128<(5 - DN) * CV_KROW + 64>(haddr); SB;
    MF(accH[3], c.w5, c.h1); MF(accL[0], c.w2, c.l1); SB; n.l0 = dsr128<(5 - DN) * CV_KROW>(laddr); SB;
    MF(accL[1], c.w3, c.l1); MF(accL[2], c.w4, c.l1); SB; n.l1 = dsr128<(5 - DN) * CV_KROW + 64>(laddr); SB;
    MF(accL[3], c.w5, c.l1); SB;
  } else {
    MF(accH[0], w0, c.h0); SB; n.w2 = ldW<4 * DN + 2>(aaddr); SB;
    MF(accH[1], w1, c.h0); SB; n.w3 = ldW<4 * DN + 3>(aaddr); SB;
    MF(accH[2], c.w2, c.h0); SB; n.w4 = ldW<4 * DN + 4>(aaddr); SB;
    MF(accH[3], c.w3, c.h0); SB; n.w5 = ldW<4 * DN + 5>(aaddr); SB;
    MF(accH[0], c.w2, c.h1); SB; n.h0 = dsr128<(5 - DN) * CV_KROW>(haddr); SB;
    MF(accH[1], c.w3, c.h1); SB; n.h1 = dsr128<(5 - DN) * CV_KROW + 64>(haddr); SB;
    MF(accH[2], c.w4, c.h1); MF(accH[3], c.w5, c.h1); SB;
  }
  w0 = c.w4; w1 = c.w5;
}
template <int DL> __device__ __forceinline__ void conv_load0(ConvStep& st, u32 aaddr, u32 haddr, u32 laddr) {
  st.w2 = ldW<4 * DL + 2>(aaddr); st.w3 = ldW<4 * DL + 3>(aaddr);
  st.w4 = ldW<4 * DL + 4>(aaddr); st.w5 = ldW<4 * DL + 5>(aaddr);
  st.h0 = dsr128<(5 - DL) * CV_KROW>(haddr); st.h1 = dsr128<(5 - DL) * CV_KROW + 64>(haddr);
  st.l0 = dsr128<(5 - DL) * CV_KROW>(laddr); st.l1 = dsr128<(5 - DL) * CV_KROW + 64>(laddr);
}
__device__ __forceinline__ void conv_main(int nb1, int nb2, u32 aaddr, u32 haddr, u32 laddr, f32x4 (&accH)[4], f32x4 (&accL)[4]) {
  ConvStep st[4];
  bf16x8 w0 = ldW<0>(aaddr), w1 = ldW<1>(aaddr);
  conv_load0<0>(st[0], aaddr, haddr, laddr);
  conv_load0<1>(st[1], aaddr, haddr, laddr);
#pragma unroll 1
  for (int i = 0; i < nb1; ++i) {
    conv_step<0, true>(st, w0, w1, aaddr, haddr, laddr, accH, accL);
    conv_step<1, true>(st, w0, w1, aaddr, haddr, laddr, accH, accL);
    conv_step<2, true>(st, w0, w1, aaddr, haddr, laddr, accH, accL);
    conv_step<3, true>(st, w0, w1, aaddr, haddr, laddr, accH, accL);
    aaddr += 4 * 128; haddr -= 4 * CV_KROW; laddr -= 4 * CV_KROW;
  }
#pragma unroll 1
  for (int i = 0; i < nb2; ++i) {
    conv_step<0, false>(st, w0, w1, aaddr, haddr, laddr, accH, accL);
    conv_step<1, false>(st, w0, w1, aaddr, haddr, laddr, accH, accL);
    conv_step<2, false>(st, w0, w1, aaddr, haddr, laddr, accH, accL);
    conv_step<3, false>(st, w0, w1, aaddr, haddr, laddr, accH, accL);
    aaddr += 4 * 128; haddr -= 4 * CV_KROW;
  }
  asm volatile("s_waitcnt lgkmcnt(0)" ::: "memory");
}

__device__ __forceinline__ float gelu_tanh(float g) {
  const float c0 = 2.0f * 0.7978845608028654f * 1.4426950408889634f;
  const float c1 = c0 * 0.044715f;
  float g2 = g * g;
  float z = g * __builtin_fmaf(g2, c1, c0);
  float e = __builtin_amdgcn_exp2f(z);
  float r = __builtin_amdgcn_rcpf(1.0f + e);
  return __builtin_fmaf(-g, r, g);
}

__global__ __launch_bounds__(256, 2) void k2_kernel(const u16* __restrict__ dskip, char* __restrict__ ws) {
  __shared__ __attribute__((aligned(16))) char smem[K2_LDS];
  const int tid = threadIdx.x;
  const int lane = tid & 63;
  const int wave = __builtin_amdgcn_readfirstlane(tid >> 6);
  const int b = blockIdx.x;
  STAMP(1, 0);

  if (b < 256) {
    const int tt = ((b & 7) >> 2) * 8 + (b >> 5);
    const int fg = (b & 3) * 4 + ((b >> 3) & 3);
    const u16* w1 = (const u16*)(ws + WS_W1F) + (size_t)fg * 16 * 14 * 512;
    const u16* xf = (const u16*)(ws + WS_XNF) + (size_t)(tt * 8) * 16 * 512;
    f32x4 acc[14][2];
#pragma unroll
    for (int i = 0; i < 14; ++i) { acc[i][0] = (f32x4){0.f, 0.f, 0.f, 0.f}; acc[i][1] = (f32x4){0.f, 0.f, 0.f, 0.f}; }
    const u16* gsrc[6];
    int gdst[6];
    int gstep[6];
#pragma unroll
    for (int i = 0; i < 6; ++i) {
      int p = wave + 4 * i;
      if (p >= 22) p -= 22;
      if (p < 14) { gsrc[i] = w1 + p * 512 + lane * 8; gstep[i] = 14 * 512; }
      else { gsrc[i] = xf + (p - 14) * 16 * 512 + lane * 8; gstep[i] = 512; }
      gdst[i] = p * 1024 + lane * 16;
    }
    auto issue1 = [&](int ks, int slot, int i) {
      char* sb = smem + slot * G1_STAGE;
      __builtin_amdgcn_global_load_lds((const __attribute__((address_space(1))) u32*)(gsrc[i] + ks * gstep[i]),
                                       (__attribute__((address_space(3))) u32*)(sb + gdst[i]), 16, 0, 0);
    };
#pragma unroll
    for (int i = 0; i < 6; ++i) issue1(0, 0, i);
#pragma unroll
    for (int i = 0; i < 6; ++i) issue1(1, 1, i);
#pragma unroll
    for (int i = 0; i < 6; ++i) issue1(2, 2, i);
    asm volatile("s_waitcnt vmcnt(12)" ::: "memory");
    __builtin_amdgcn_s_barrier();
    bf16x8 alo[7], ahi[7], t0, t1;
    {
      const char* buf = smem + lane * 16;
      t0 = *(const bf16x8*)(buf + (14 + 2 * wave) * 1024);
      t1 = *(const bf16x8*)(buf + (15 + 2 * wave) * 1024);
#pragma unroll
      for (int t = 0; t < 7; ++t) alo[t] = *(const bf16x8*)(buf + t * 1024);
    }
    int slot = 0;
#pragma unroll 1
    for (int ks = 0; ks < 16; ++ks) {
      const int nslot = slot == 2 ? 0 : slot + 1;
      const char* buf = smem + slot * G1_STAGE + lane * 16;
#pragma unroll
      for (int t = 0; t < 7; ++t) {
        acc[t][0] = __builtin_amdgcn_mfma_f32_16x16x32_bf16(alo[t], t0, acc[t][0], 0, 0, 0);
        acc[t][1] = __builtin_amdgcn_mfma_f32_16x16x32_bf16(alo[t], t1, acc[t][1], 0, 0, 0);
        __builtin_amdgcn_sched_barrier(0);
        ahi[t] = *(const bf16x8*)(buf + (7 + t) * 1024);
        __builtin_amdgcn_sched_barrier(0);
      }
      bf16x8 n0, n1;
      asm volatile("s_waitcnt vmcnt(6) lgkmcnt(0)" ::: "memory");
      __builtin_amdgcn_s_barrier();
      __builtin_amdgcn_sched_barrier(0);
      const char* nb = smem + nslot * G1_STAGE + lane * 16;
      const int kn = ks + 3 < 16 ? ks + 3 : 15;
#pragma unroll
      for (int t = 0; t < 7; ++t) {
        acc[7 + t][0] = __builtin_amdgcn_mfma_f32_16x16x32_bf16(ahi[t], t0, acc[7 + t][0], 0, 0, 0);
        __builtin_amdgcn_sched_barrier(0);
        alo[t] = *(const bf16x8*)(nb + t * 1024);
        if (t == 0) n0 = *(const bf16x8*)(nb + (14 + 2 * wave) * 1024);
        if (t == 1) n1 = *(const bf16x8*)(nb + (15 + 2 * wave) * 1024);
        __builtin_amdgcn_sched_barrier(0);
        acc[7 + t][1] = __builtin_amdgcn_mfma_f32_16x16x32_bf16(ahi[t], t1, acc[7 + t][1], 0, 0, 0);
        if (t < 6) {
          __builtin_amdgcn_sched_barrier(0);
          issue1(kn, slot, t);
        }
        __builtin_amdgcn_sched_barrier(0);
      }
      t0 = n0; t1 = n1;
      slot = nslot;
    }
    asm volatile("s_waitcnt vmcnt(0) lgkmcnt(0)" ::: "memory");
    STAMP(1, 2);
    u16* hf = (u16*)(ws + WS_HF);
    const int c = lane & 15, Q = lane >> 4;
#pragma unroll
    for (int t = 0; t < 7; ++t) {
      int ft = fg * 7 + t;
      int ks2 = ft >> 1;
      int q = 2 * (ft & 1) + (Q >> 1);
#pragma unroll
      for (int m = 0; m < 2; ++m) {
        float hv[4];
#pragma unroll
        for (int r = 0; r < 4; ++r) {
          float g = rbf(acc[t][m][r]);
          float u = rbf(acc[t + 7][m][r]);
          hv[r] = rbf(gelu_tanh(g)) * u;
        }
        int tile = tt * 8 + 2 * wave + m;
        u32x2 o = {pack2(hv[0], hv[1]), pack2(hv[2], hv[3])};
        GST2(u32x2, hf + (((size_t)tile * 56 + ks2) * 64 + (c + 16 * q)) * 8 + 4 * (Q & 1), o);
      }
    }
    STAMP(1, 3);
    return;
  }

  {
    const int chl = wave >> 1;
    const int d = (b - 256) * 2 + chl;
    const int t128 = tid & 127;
    char* cb = smem + chl * CV_CH;
    const u16* ut = (const u16*)(ws + WS_XNT) + (size_t)d * L_;
    const u16* kc = (const u16*)(ws + WS_KC) + (size_t)d * 2 * L_;
    char* kb = cb + 4 * CV_UCOPY;
    {
      u32x4 v0[3], v1[3], kv[4];
#pragma unroll
      for (int it = 0; it < 3; ++it) {
        int cch = it * 128 + t128 - 8;
        int c0 = cch < 0 ? 0 : (cch > 255 ? 255 : cch);
        int c1 = cch + 1 < 0 ? 0 : (cch + 1 > 255 ? 255 : cch + 1);
        v0[it] = *(const u32x4*)(ut + c0 * 8);
        v1[it] = *(const u32x4*)(ut + c1 * 8);
      }
#pragma unroll
      for (int it = 0; it < 4; ++it) {
        int pc = it * 128 + t128;
        kv[it] = *(const u32x4*)(kc + pc * 8);
      }
      __builtin_amdgcn_sched_barrier(0);
#pragma unroll
      for (int it = 0; it < 3; ++it) {
        int X = it * 128 + t128;
        int cch = X - 8;
        bool ok0 = (cch >= 0 && cch < 256), ok1 = (cch + 1 >= 0 && cch + 1 < 256);
        u32 w[6] = {ok0 ? v0[it][0] : 0u, ok0 ? v0[it][1] : 0u, ok0 ? v0[it][2] : 0u, ok0 ? v0[it][3] : 0u,
                    ok1 ? v1[it][0] : 0u, ok1 ? v1[it][1] : 0u};
        u32 s1[5];
#pragma unroll
        for (int e = 0; e < 5; ++e) s1[e] = __builtin_amdgcn_alignbit(w[e + 1], w[e], 16);
        if (X < 276) {
          *(u32x4*)(cb + 0 * CV_UCOPY + X * 16) = (u32x4){w[0], w[1], w[2], w[3]};
          *(u32x4*)(cb + 1 * CV_UCOPY + X * 16) = (u32x4){s1[0], s1[1], s1[2], s1[3]};
          *(u32x4*)(cb + 2 * CV_UCOPY + X * 16) = (u32x4){w[1], w[2], w[3], w[4]};
          *(u32x4*)(cb + 3 * CV_UCOPY + X * 16) = (u32x4){s1[1], s1[2], s1[3], s1[4]};
        }
      }
#pragma unroll
      for (int it = 0; it < 4; ++it) {
        int pc = it * 128 + t128;
        int arr = pc >> 8, row = (pc >> 3) & 31, ch = pc & 7;
        *(u32x4*)(kb + arr * CV_KARR + (row + 7) * CV_KROW + ch * 16) = kv[it];
      }
    }
    if (t128 < 112) {
      int arr = t128 / 56, r = t128 % 56;
      *(uint4*)(kb + arr * CV_KARR + (r >> 3) * CV_KROW + (r & 7) * 16) = make_uint4(0, 0, 0, 0);
    }
    __syncthreads();
    STAMP(1, 1);

    const int p = wave & 1;
    const int gH = p ? 2 : 3, gL = p ? 1 : 0;
    const int a = lane & 15, q = lane >> 4;
    const char* abase = cb + ((a + 1) & 3) * CV_UCOPY + (8 * q + 4 * ((a + 1) >> 2)) * 2;
    const char* bH = kb + (a >= 8 ? CV_KARR : 0) + (8 * gH + (a & 7) + 7) * CV_KROW + 16 * q;
    const char* bL = kb + (a >= 8 ? CV_KARR : 0) + (8 * gL + (a & 7) + 7) * CV_KROW + 16 * q;
    f32x4 accH[4], accL[4];
#pragma unroll
    for (int i = 0; i < 4; ++i) { accH[i] = (f32x4){0.f, 0.f, 0.f, 0.f}; accL[i] = (f32x4){0.f, 0.f, 0.f, 0.f}; }
    const int col = (a < 8) ? (8 * gH + a) : (8 * gL + a - 8);
    u32x2 uvp[4];
#pragma unroll
    for (int rt = 0; rt < 4; ++rt) {
      int t0 = 64 * col + 16 * rt + 4 * q;
      uvp[rt] = *(const u32x2*)(ut + t0);
    }
    const float dk = bf2f(dskip[d]);
    __builtin_amdgcn_sched_barrier(0);
    {
      typedef __attribute__((address_space(3))) const char* lptr;
      u32 aaddr = (u32)(size_t)(lptr)abase;
      u32 haddr = (u32)(size_t)(lptr)bH - 5 * CV_KROW;
      u32 laddr = (u32)(size_t)(lptr)bL - 5 * CV_KROW;
      conv_main(p ? 4 : 2, p ? 2 : 6, aaddr, haddr, laddr, accH, accL);
    }
    STAMP(1, 2);
    u16* r1 = (u16*)(ws + WS_R1T) + (size_t)d * L_;
#pragma unroll
    for (int rt = 0; rt < 4; ++rt) {
      int t0 = 64 * col + 16 * rt + 4 * q;
      float o[4];
#pragma unroll
      for (int r = 0; r < 4; ++r) {
        float mine = (a < 8) ? accH[rt][r] : accL[rt][r];
        float give = (a < 8) ? accL[rt][r] : accH[rt][r];
        float other = __int_as_float(__builtin_amdgcn_update_dpp(0, __float_as_int(give), 0x128, 0xf, 0xf, true));
        float y = rbf(mine + other);
        float un = bf2f((u16)(uvp[rt][r >> 1] >> (16 * (r & 1))));
        o[r] = y + rbf(un * dk);
      }
      GST2(u32x2, r1 + t0, ((u32x2){pack2(o[0], o[1]), pack2(o[2], o[3])}));
    }
    STAMP(1, 3);
  }
}

__global__ __launch_bounds__(256) void k3_kernel(const u16* __restrict__ x, char* __restrict__ ws, u16* __restrict__ out) {
  __shared__ __attribute__((aligned(16))) f32x4 red[4][16][64];
  const int tid = threadIdx.x;
  const int lane = tid & 63;
  const int wave = __builtin_amdgcn_readfirstlane(tid >> 6);
  const int b = blockIdx.x;
  STAMP(2, 0);
  const int tb = (b & 7) * 4 + ((b >> 3) & 3);
  const int db = b >> 5;
  const u16* ha = (const u16*)(ws + WS_HF) + ((size_t)(tb * 4) * 56 + wave * 14) * 512 + lane * 8;
  const u16* wb = (const u16*)(ws + WS_W2F) + ((size_t)(db * 4) * 56 + wave * 14) * 512 + lane * 8;
  f32x4 acc[4][4];
#pragma unroll
  for (int m = 0; m < 4; ++m)
#pragma unroll
    for (int n = 0; n < 4; ++n) acc[m][n] = (f32x4){0.f, 0.f, 0.f, 0.f};
  constexpr int PF = 4;
  bf16x8 A[PF][4], B[PF][4];
#pragma unroll
  for (int s = 0; s < PF; ++s)
#pragma unroll
    for (int m = 0; m < 4; ++m) {
      A[s][m] = *(const bf16x8*)(ha + ((size_t)m * 56 + s) * 512);
      B[s][m] = *(const bf16x8*)(wb + ((size_t)m * 56 + s) * 512);
    }
  __builtin_amdgcn_sched_barrier(0);
#pragma unroll
  for (int s = 0; s < 14; ++s) {
    const int sl = s % PF;
#pragma unroll
    for (int m = 0; m < 4; ++m)
#pragma unroll
      for (int n = 0; n < 4; ++n)
        acc[m][n] = __builtin_amdgcn_mfma_f32_16x16x32_bf16(A[sl][m], B[sl][n], acc[m][n], 0, 0, 0);
    __builtin_amdgcn_sched_barrier(0);
    if (s + PF < 14) {
#pragma unroll
      for (int m = 0; m < 4; ++m) {
        A[sl][m] = *(const bf16x8*)(ha + ((size_t)m * 56 + s + PF) * 512);
        B[sl][m] = *(const bf16x8*)(wb + ((size_t)m * 56 + s + PF) * 512);
      }
    }
    __builtin_amdgcn_sched_barrier(0);
  }
  STAMP(2, 1);
  const int c = lane & 15, Q = lane >> 4;
  const u16* at = (const u16*)(ws + WS_R1T);
  const int tok0 = tb * 64 + wave * 16 + 4 * Q;
  u32x2 av[4];
#pragma unroll
  for (int n = 0; n < 4; ++n) av[n] = *(const u32x2*)(at + (size_t)(db * 64 + n * 16 + c) * L_ + tok0);
  const int orow = tb * 64 + wave * 16 + (lane >> 2);
  const int ocol = db * 64 + (lane & 3) * 16;
  u32x4 xv0 = *(const u32x4*)(x + (size_t)orow * D_ + ocol);
  u32x4 xv1 = *(const u32x4*)(x + (size_t)orow * D_ + ocol + 8);
#pragma unroll
  for (int m = 0; m < 4; ++m)
#pragma unroll
    for (int n = 0; n < 4; ++n) red[wave][m * 4 + n][lane] = acc[m][n];
  __syncthreads();
  STAMP(2, 2);
  float dn[4][4];
#pragma unroll
  for (int n = 0; n < 4; ++n) {
    f32x4 v = red[0][wave * 4 + n][lane];
#pragma unroll
    for (int w = 1; w < 4; ++w) {
      f32x4 t = red[w][wave * 4 + n][lane];
      v[0] += t[0]; v[1] += t[1]; v[2] += t[2]; v[3] += t[3];
    }
#pragma unroll
    for (int r = 0; r < 4; ++r) dn[n][r] = v[r];
  }
  __syncthreads();
  u32* tp = (u32*)red + wave * (16 * 68);
#pragma unroll
  for (int n = 0; n < 4; ++n)
#pragma unroll
    for (int r = 0; r < 4; ++r) {
      u32 a16 = (r & 1) ? (av[n][r >> 1] >> 16) : (av[n][r >> 1] & 0xffffu);
      tp[(4 * Q + r) * 68 + n * 16 + c] = a16 | ((u32)f2bf(dn[n][r]) << 16);
    }
  __builtin_amdgcn_s_waitcnt(0xC07F);
  __builtin_amdgcn_wave_barrier();
  {
    const u32* rp = tp + (lane >> 2) * 68 + (lane & 3) * 16;
    u32x4 o[2];
#pragma unroll
    for (int h = 0; h < 2; ++h) {
      u32x4 p0 = *(const u32x4*)(rp + h * 8), p1 = *(const u32x4*)(rp + h * 8 + 4);
      u32x4 xv = h ? xv1 : xv0;
#pragma unroll
      for (int e = 0; e < 4; ++e) {
        u32 q0 = e < 2 ? p0[2 * e] : p1[2 * e - 4], q1 = e < 2 ? p0[2 * e + 1] : p1[2 * e - 3];
        u32 r1 = pack2(lo16(xv[e]) + lo16(q0), hi16(xv[e]) + lo16(q1));
        o[h][e] = pack2(lo16(r1) + hi16(q0), hi16(r1) + hi16(q1));
      }
    }
    *(u32x4*)(out + (size_t)orow * D_ + ocol) = o[0];
    *(u32x4*)(out + (size_t)orow * D_ + ocol + 8) = o[1];
  }
  STAMP(2, 3);
}

extern "C" void kernel_launch(void* const* d_in, const int* in_sizes, int n_in, void* d_out, int out_size,
                              void* d_ws, size_t ws_size, hipStream_t stream) {
  const u16* x = (const u16*)d_in[0];
  const u16* logA = (const u16*)d_in[1];
  const u16* Aim = (const u16*)d_in[2];
  const u16* Cre = (const u16*)d_in[3];
  const u16* Cim = (const u16*)d_in[4];
  const u16* logdt = (const u16*)d_in[5];
  const u16* dskip = (const u16*)d_in[6];
  const u16* wg = (const u16*)d_in[8];
  const u16* wu = (const u16*)d_in[9];
  const u16* wd = (const u16*)d_in[10];
  char* ws = (char*)d_ws;
  k1_kernel<<<K1_GRID, 512, 0, stream>>>(x, logA, Aim, Cre, Cim, logdt, wg, wu, wd, ws);
  k2_kernel<<<512, 256, 0, stream>>>(dskip, ws);
  k3_kernel<<<256, 256, 0, stream>>>(x, ws, (u16*)d_out);
}
```

```cpp
#include <hip/hip_runtime.h>
#include <hip/hip_bf16.h>

typedef __attribute__((ext_vector_type(8))) short bf16x8;
typedef __attribute__((ext_vector_type(4))) float f32x4;
typedef unsigned short u16;
typedef unsigned int u32;
typedef __attribute__((ext_vector_type(4))) unsigned int u32x4;
typedef __attribute__((ext_vector_type(2))) unsigned int u32x2;

constexpr int L_ = 2048;
constexpr int D_ = 512;
constexpr int N_ = 128;
constexpr int F_ = 1792;

constexpr size_t WS_XNF = 0;
constexpr size_t WS_XNT = WS_XNF + (size_t)L_ * D_ * 2;
constexpr size_t WS_W1F = WS_XNT + (size_t)L_ * D_ * 2;
constexpr size_t WS_W2F = WS_W1F + (size_t)D_ * F_ * 2 * 2;
constexpr size_t WS_KC = WS_W2F + (size_t)D_ * F_ * 2;
constexpr size_t WS_HF = WS_KC + (size_t)D_ * 2 * L_ * 2;
constexpr size_t WS_R1T = WS_HF + (size_t)L_ * F_ * 2;
constexpr size_t WS_END = WS_R1T + (size_t)D_ * L_ * 2;

template <typename T> __device__ __forceinline__ void gst_sc1(T* p, T v);
template <> __device__ __forceinline__ void gst_sc1<u32x4>(u32x4* p, u32x4 v) { asm volatile("global_store_dwordx4 %0, %1, off sc1\n\ts_nop 1" :: "v"(p), "v"(v) : "memory"); }
template <> __device__ __forceinline__ void gst_sc1<u32x2>(u32x2* p, u32x2 v) { asm volatile("global_store_dwordx2 %0, %1, off sc1\n\ts_nop 1" :: "v"(p), "v"(v) : "memory"); }
#define GST(T, p, v) gst_sc1<T>((T*)(p), (T)(v))
#define GST2 GST
__device__ __forceinline__ float bf2f(u16 v) { return __uint_as_float(((u32)v) << 16); }
__device__ __forceinline__ u16 f2bf(float f) {
  __bf16 b = (__bf16)f;
  return __builtin_bit_cast(u16, b);
}
__device__ __forceinline__ float rbf(float f) { return bf2f(f2bf(f)); }
__device__ __forceinline__ u32 pack2(float lo, float hi) {
  typedef __attribute__((ext_vector_type(2))) __bf16 bf2;
  bf2 v = {(__bf16)lo, (__bf16)hi};
  return __builtin_bit_cast(u32, v);
}
__device__ __forceinline__ float lo16(u32 w) { return __uint_as_float(w << 16); }
__device__ __forceinline__ float hi16(u32 w) { return __uint_as_float(w & 0xffff0000u); }

__device__ __forceinline__ float wave_sum(float v) {
  int x = __float_as_int(v);
  v += __int_as_float(__builtin_amdgcn_update_dpp(0, x, 0xB1, 0xf, 0xf, true));
  x = __float_as_int(v);
  v += __int_as_float(__builtin_amdgcn_update_dpp(0, x, 0x4E, 0xf, 0xf, true));
  x = __float_as_int(v);
  v += __int_as_float(__builtin_amdgcn_update_dpp(0, x, 0x141, 0xf, 0xf, true));
  x = __float_as_int(v);
  v += __int_as_float(__builtin_amdgcn_update_dpp(0, x, 0x140, 0xf, 0xf, true));
  x = __float_as_int(v);
  float a = __int_as_float(__builtin_amdgcn_readlane(x, 0));
  float b = __int_as_float(__builtin_amdgcn_readlane(x, 16));
  float c = __int_as_float(__builtin_amdgcn_readlane(x, 32));
  float d = __int_as_float(__builtin_amdgcn_readlane(x, 48));
  return (a + b) + (c + d);
}

constexpr int K1_NB_KGEN = 512;
constexpr int K1_NB_LN = 128;
constexpr int K1_NB_W1 = 112;
constexpr int K1_NB_W2 = 56;
constexpr int K1_GRID = K1_NB_KGEN + K1_NB_LN + K1_NB_W1 + K1_NB_W2;

__global__ __launch_bounds__(512) void k1_kernel(
    const u16* __restrict__ x, const u16* __restrict__ logA, const u16* __restrict__ Aim,
    const u16* __restrict__ Cre, const u16* __restrict__ Cim, const u16* __restrict__ logdt,
    const u16* __restrict__ wg, const u16* __restrict__ wu, const u16* __restrict__ wd,
    char* __restrict__ ws) {
  __shared__ __attribute__((aligned(16))) char smem[128 * 136 * 2];
  const int tid = threadIdx.x;
  const int lane = tid & 63;
  const int wave = tid >> 6;
  int b = blockIdx.x;

  if (b < K1_NB_KGEN) {
    const int d = b;
    f32x4* prm = (f32x4*)smem;
    float* tab = (float*)(smem + 4096);
    if (tid < N_) {
      float dt = rbf(expf(bf2f(logdt[d])));
      float ar = rbf(expf(bf2f(logA[d * N_ + tid])));
      float zr = -ar * dt;
      float zi = bf2f(Aim[d * N_ + tid]) * dt;
      const float I_HI = 0.15915494309189535f;
      const float I_LO = 6.4206382e-9f;
      float zh = zi * I_HI;
      float zl = __builtin_fmaf(zi, I_LO, __builtin_fmaf(zi, I_HI, -zh));
      prm[2 * tid] = (f32x4){zr * 1.4426950408889634f, zh, zl, 0.f};
      float cr = bf2f(Cre[d * N_ + tid]) * dt, ci = bf2f(Cim[d * N_ + tid]) * dt;
      prm[2 * tid + 1] = (f32x4){cr, ci, -cr, -ci};
    }
    __syncthreads();
    {
      const int i = lane & 15, kk = lane >> 4;
      const float pa0 = (float)((i & 7) + 128 * (i >> 3));
      const float pa1 = (float)(i < 8 ? 256 + 128 * (i >> 2) + 2 * (i & 3) : 512 + 128 * ((i - 8) >> 1) + 4 * (i & 1));
      const float pa2 = (float)(1024 + 128 * (i > 8 ? 8 : i));
      const float pb0 = (float)(8 * i);
      f32x4 acc[3];
#pragma unroll
      for (int sc = 0; sc < 3; ++sc) acc[sc] = (f32x4){0.f, 0.f, 0.f, 0.f};
      auto cexp = [&](const f32x4& z, float pp, float& er, float& ei) {
        float e = __builtin_amdgcn_exp2f(z[0] * pp);
        float r = __builtin_rintf(z[1] * pp);
        float f = __builtin_fmaf(z[1], pp, -r);
        f = __builtin_fmaf(z[2], pp, f);
        er = e * __builtin_amdgcn_cosf(f);
        asm("" : "+v"(er));
        ei = e * __builtin_amdgcn_sinf(f);
        asm("" : "+v"(ei));
      };
      auto split = [&](float re, float im, u32& hi, u32& lo) {
        hi = pack2(re, im);
        lo = pack2(re - lo16(hi), im - hi16(hi));
      };
      {
        u32x4 ah[3], al[3], bh, bl;
#pragma unroll
        for (int m = 0; m < 4; ++m) {
          const int n = 16 * wave + 4 * kk + m;
          f32x4 z = prm[2 * n], cc = prm[2 * n + 1];
          float er, ei;
          u32 h, l;
          cexp(z, pa0, er, ei); split(er, ei, h, l); ah[0][m] = h; al[0][m] = l;
          cexp(z, pa1, er, ei); split(er, ei, h, l); ah[1][m] = h; al[1][m] = l;
          cexp(z, pa2, er, ei); split(er, ei, h, l); ah[2][m] = h; al[2][m] = l;
          cexp(z, pb0, er, ei);
          float t0 = cc[1] * ei;
          asm("" : "+v"(t0));
          float bre = __builtin_fmaf(cc[0], er, -t0);
          float t1 = cc[2] * ei;
          asm("" : "+v"(t1));
          float bim = __builtin_fmaf(cc[3], er, t1);
          split(bre, bim, h, l); bh[m] = h; bl[m] = l;
        }
#pragma unroll
        for (int sc = 0; sc < 3; ++sc) {
          acc[sc] = __builtin_amdgcn_mfma_f32_16x16x32_bf16(__builtin_bit_cast(bf16x8, al[sc]), __builtin_bit_cast(bf16x8, bh), acc[sc], 0, 0, 0);
          acc[sc] = __builtin_amdgcn_mfma_f32_16x16x32_bf16(__builtin_bit_cast(bf16x8, ah[sc]), __builtin_bit_cast(bf16x8, bl), acc[sc], 0, 0, 0);
          acc[sc] = __builtin_amdgcn_mfma_f32_16x16x32_bf16(__builtin_bit_cast(bf16x8, ah[sc]), __builtin_bit_cast(bf16x8, bh), acc[sc], 0, 0, 0);
        }
      }
      float* tw = tab + wave * 768;
#pragma unroll
      for (int r = 0; r < 4; ++r) {
        int row = 4 * kk + r;
#pragma unroll
        for (int sc = 0; sc < 3; ++sc) tw[sc * 256 + 16 * row + i] = acc[sc][r];
      }
    }
    __syncthreads();
    for (int e = tid; e < 768; e += 512) {
      float v = ((tab[e] + tab[768 + e]) + (tab[2 * 768 + e] + tab[3 * 768 + e])) +
                ((tab[4 * 768 + e] + tab[5 * 768 + e]) + (tab[6 * 768 + e] + tab[7 * 768 + e]));
      tab[e] = 2.0f * v;
    }
    __syncthreads();
    {
      u16* kc = (u16*)(ws + WS_KC) + (size_t)d * 2 * L_;
      u32x2 hi, lo;
#pragma unroll
      for (int e2 = 0; e2 < 2; ++e2) {
        float kv[2];
#pragma unroll
        for (int e1 = 0; e1 < 2; ++e1) {
          int o = tid * 4 + 2 * e2 + e1;
          int l = (o & ~63) + 63 - (o & 63);
          int p = (int)rbf((float)l);
          int j = (p & 127) >> 3;
          int row;
          if (p < 256) row = (p & 7) + 8 * (p >> 7);
          else if (p < 512) row = 16 + (((p - 256) >> 7) << 2) + ((p & 7) >> 1);
          else if (p < 1024) row = 24 + (((p - 512) >> 7) << 1) + ((p & 7) >> 2);
          else row = 32 + ((p - 1024) >> 7);
          kv[e1] = tab[row * 16 + j];
        }
        hi[e2] = pack2(kv[0], kv[1]);
        lo[e2] = pack2(kv[0] - lo16(hi[e2]), kv[1] - hi16(hi[e2]));
      }
      GST(u32x2, kc + tid * 4, hi);
      GST(u32x2, kc + L_ + tid * 4, lo);
    }
    return;
  }
  b -= K1_NB_KGEN;
  __builtin_amdgcn_s_setprio(3);
  if (b < K1_NB_LN) {
    constexpr int LDW = 520;
    u16* sn = (u16*)smem;
    const int row0 = b * 16;
    u32x4 v[2];
#pragma unroll
    for (int r = 0; r < 2; ++r) v[r] = *(const u32x4*)(x + (size_t)(row0 + wave * 2 + r) * D_ + lane * 8);
#pragma unroll
    for (int r = 0; r < 2; ++r) {
      float f[8];
#pragma unroll
      for (int e = 0; e < 4; ++e) { f[2 * e] = lo16(v[r][e]); f[2 * e + 1] = hi16(v[r][e]); }
      float s = 0.f;
#pragma unroll
      for (int e = 0; e < 8; ++e) s += f[e];
      s = wave_sum(s);
      float mean = s * (1.0f / D_);
      float q = 0.f;
#pragma unroll
      for (int e = 0; e < 8; ++e) { float dd = f[e] - mean; q += dd * dd; }
      q = wave_sum(q);
      float var = q * (1.0f / D_);
      float mb = rbf(mean);
      float vb = rbf(var);
      float den = rbf(sqrtf(rbf(vb + 1e-5f)));
      float rinv = 1.0f / den;
      u32x4 ow;
#pragma unroll
      for (int e = 0; e < 4; ++e) {
        u32 dp = pack2(f[2 * e] - mb, f[2 * e + 1] - mb);
        ow[e] = pack2(lo16(dp) * rinv, hi16(dp) * rinv);
      }
      int lr = wave * 2 + r;
      *(u32x4*)(sn + lr * LDW + lane * 8) = ow;
    }
    __syncthreads();
    u16* xnf = (u16*)(ws + WS_XNF) + (size_t)b * 16 * 512;
#pragma unroll
    for (int it = 0; it < 2; ++it) {
      int pc = it * 512 + tid;
      int ln = pc & 63, ks = pc >> 6;
      int c = ln & 15, q = ln >> 4;
      u32x4 val = *(const u32x4*)(sn + c * LDW + ks * 32 + q * 8);
      GST(u32x4, xnf + (size_t)pc * 8, val);
    }
    {
      u16* xnt = (u16*)(ws + WS_XNT);
      const int cp = tid >> 1, hh = tid & 1;
      u32 w[8];
#pragma unroll
      for (int r = 0; r < 8; ++r) w[r] = *(const u32*)(sn + (8 * hh + r) * LDW + 2 * cp);
      u32x4 lo0, hi0;
#pragma unroll
      for (int e = 0; e < 4; ++e) {
        lo0[e] = __builtin_amdgcn_perm(w[2 * e + 1], w[2 * e], 0x05040100);
        hi0[e] = __builtin_amdgcn_perm(w[2 * e + 1], w[2 * e], 0x07060302);
      }
      u16* d0 = xnt + (size_t)(2 * cp) * L_ + row0 + 8 * hh;
      GST(u32x4, d0, lo0);
      GST(u32x4, d0 + L_, hi0);
    }
    return;
  }
  b -= K1_NB_LN;
  {
    constexpr int LDW = 136;
    u16* st = (u16*)smem;
    const u16* src;
    int ld, ks0, nch;
    int kind;
    if (b < K1_NB_W1) {
      kind = b / 56;
      int r = b % 56;
      ks0 = (r / 14) * 4; nch = r % 14;
      src = kind == 0 ? wg : wu;
      ld = F_;
    } else {
      int r = b - K1_NB_W1;
      kind = 2;
      ks0 = (r >> 2) * 4; nch = r & 3;
      src = wd;
      ld = D_;
    }
    u32x4 val[4];
#pragma unroll
    for (int h = 0; h < 4; ++h) {
      int r = (tid >> 4) + 32 * h, cc = (tid & 15) * 8;
      val[h] = *(const u32x4*)(src + (size_t)(ks0 * 32 + r) * ld + nch * 128 + cc);
    }
#pragma unroll
    for (int h = 0; h < 4; ++h) {
      int r = (tid >> 4) + 32 * h, cc = (tid & 15) * 8;
      *(u32x4*)(st + r * LDW + cc) = val[h];
    }
    __syncthreads();
#pragma unroll
    for (int h = 0; h < 4; ++h) {
      int ks = ks0 + h;
      int ln = tid & 63, nt = tid >> 6;
      int a = ln & 15, q = ln >> 4;
      const u16* sr = st + (h * 32 + 8 * q) * LDW + nt * 16 + a;
      u32x4 w;
#pragma unroll
      for (int e = 0; e < 4; ++e) w[e] = (u32)sr[(2 * e) * LDW] | ((u32)sr[(2 * e + 1) * LDW] << 16);
      int ft = nch * 8 + nt;
      u16* dst;
      if (kind < 2) {
        int fg = ft / 7, tau = ft % 7 + 7 * kind;
        dst = (u16*)(ws + WS_W1F) + ((size_t)(fg * 16 + ks) * 14 + tau) * 512 + ln * 8;
      } else {
        dst = (u16*)(ws + WS_W2F) + ((size_t)ft * 56 + ks) * 512 + ln * 8;
      }
      GST(u32x4, dst, w);
    }
  }
}

constexpr int CV_UCOPY = 4416;
constexpr int CV_KROW = 160;
constexpr int CV_KARR = 6400;
constexpr int CV_CH = 4 * CV_UCOPY + 2 * CV_KARR;
constexpr int G1_STAGE = 22 * 1024;
constexpr int K2_LDS = 3 * G1_STAGE;

#define MF(acc, A, B) acc = __builtin_amdgcn_mfma_f32_16x16x32_bf16(A, B, acc, 0, 0, 0)
template <int OFF> __device__ __forceinline__ u32x2 dsr64(u32 addr) {
  u32x2 r;
  asm volatile("ds_read_b64 %0, %1 offset:%2" : "=v"(r) : "v"(addr), "i"(OFF));
  return r;
}
template <int OFF> __device__ __forceinline__ bf16x8 dsr128(u32 addr) {
  bf16x8 r;
  asm volatile("ds_read_b128 %0, %1 offset:%2" : "=v"(r) : "v"(addr), "i"(OFF));
  return r;
}
template <int M> __device__ __forceinline__ bf16x8 ldW(u32 aaddr) {
  u32x2 x0 = dsr64<M * 32>(aaddr);
  u32x2 x1 = dsr64<M * 32 + 8>(aaddr);
  u32x4 v = {x0[0], x0[1], x1[0], x1[1]};
  return __builtin_bit_cast(bf16x8, v);
}
template <int N> __device__ __forceinline__ void wait_lgkm() {
  asm volatile("s_waitcnt lgkmcnt(%0)" ::"i"(N) : "memory");
  __builtin_amdgcn_sched_barrier(0);
}
struct ConvStep { bf16x8 w2, w3, w4, w5, h0, h1, l0, l1; };
template <int DL, int NDL> __device__ __forceinline__ void conv_load(ConvStep& st, u32 aaddr, u32 haddr, u32 laddr) {
  st.w2 = ldW<4 * DL + 2>(aaddr); st.w3 = ldW<4 * DL + 3>(aaddr);
  st.w4 = ldW<4 * DL + 4>(aaddr); st.w5 = ldW<4 * DL + 5>(aaddr);
  st.h0 = dsr128<(31 - DL) * CV_KROW>(haddr); st.h1 = dsr128<(31 - DL) * CV_KROW + 64>(haddr);
  if constexpr (DL < NDL) {
    st.l0 = dsr128<(31 - DL) * CV_KROW>(laddr); st.l1 = dsr128<(31 - DL) * CV_KROW + 64>(laddr);
  }
}
#define SB __builtin_amdgcn_sched_barrier(0)
template <int DL, int NDH, int NDL>
__device__ __forceinline__ void conv_step(ConvStep (&st)[3], bf16x8& w0, bf16x8& w1, u32 aaddr, u32 haddr, u32 laddr,
                                          f32x4 (&accH)[4], f32x4 (&accL)[4]) {
  if constexpr (DL < NDH) {
    constexpr int CN1 = (DL + 1 < NDH) ? ((DL + 1 < NDL) ? 12 : 10) : 0;
    constexpr bool PF = (DL + 2 < NDH);
    constexpr int DN = DL + 2;
    wait_lgkm<CN1>();
    ConvStep& c = st[DL % 3];
    ConvStep& n = st[(DL + 2) % 3];
    if constexpr (DL < NDL) {
      MF(accH[0], w0, c.h0); SB; if constexpr (PF) n.w2 = ldW<4 * DN + 2>(aaddr); SB;
      MF(accH[1], w1, c.h0); MF(accH[2], c.w2, c.h0); SB; if constexpr (PF) n.w3 = ldW<4 * DN + 3>(aaddr); SB;
      MF(accH[3], c.w3, c.h0); MF(accL[0], w0, c.l0); SB; if constexpr (PF) n.w4 = ldW<4 * DN + 4>(aaddr); SB;
      MF(accL[1], w1, c.l0); MF(accL[2], c.w2, c.l0); SB; if constexpr (PF) n.w5 = ldW<4 * DN + 5>(aaddr); SB;
      MF(accL[3], c.w3, c.l0); MF(accH[0], c.w2, c.h1); SB; if constexpr (PF) n.h0 = dsr128<(31 - DN) * CV_KROW>(haddr); SB;
      MF(accH[1], c.w3, c.h1); MF(accH[2], c.w4, c.h1); SB; if constexpr (PF) n.h1 = dsr128<(31 - DN) * CV_KROW + 64>(haddr); SB;
      MF(accH[3], c.w5, c.h1); MF(accL[0], c.w2, c.l1); SB; if constexpr (PF && DN < NDL) n.l0 = dsr128<(31 - DN) * CV_KROW>(laddr); SB;
      MF(accL[1], c.w3, c.l1); MF(accL[2], c.w4, c.l1); SB; if constexpr (PF && DN < NDL) n.l1 = dsr128<(31 - DN) * CV_KROW + 64>(laddr); SB;
      MF(accL[3], c.w5, c.l1); SB;
    } else {
      MF(accH[0], w0, c.h0); SB; if constexpr (PF) n.w2 = ldW<4 * DN + 2>(aaddr); SB;
      MF(accH[1], w1, c.h0); SB; if constexpr (PF) n.w3 = ldW<4 * DN + 3>(aaddr); SB;
      MF(accH[2], c.w2, c.h0); SB; if constexpr (PF) n.w4 = ldW<4 * DN + 4>(aaddr); SB;
      MF(accH[3], c.w3, c.h0); SB; if constexpr (PF) n.w5 = ldW<4 * DN + 5>(aaddr); SB;
      MF(accH[0], c.w2, c.h1); SB; if constexpr (PF) n.h0 = dsr128<(31 - DN) * CV_KROW>(haddr); SB;
      MF(accH[1], c.w3, c.h1); SB; if constexpr (PF) n.h1 = dsr128<(31 - DN) * CV_KROW + 64>(haddr); SB;
      MF(accH[2], c.w4, c.h1); MF(accH[3], c.w5, c.h1); SB;
    }
    w0 = c.w4; w1 = c.w5;
    conv_step<DL + 1, NDH, NDL>(st, w0, w1, aaddr, haddr, laddr, accH, accL);
  }
}
template <int NDH, int NDL>
__device__ __forceinline__ void conv_main(u32 aaddr, u32 haddr, u32 laddr, f32x4 (&accH)[4], f32x4 (&accL)[4]) {
  ConvStep st[3];
  bf16x8 w0 = ldW<0>(aaddr), w1 = ldW<1>(aaddr);
  conv_load<0, NDL>(st[0], aaddr, haddr, laddr);
  conv_load<1, NDL>(st[1], aaddr, haddr, laddr);
  conv_step<0, NDH, NDL>(st, w0, w1, aaddr, haddr, laddr, accH, accL);
}

__device__ __forceinline__ float gelu_tanh(float g) {
  const float c0 = 2.0f * 0.7978845608028654f * 1.4426950408889634f;
  const float c1 = c0 * 0.044715f;
  float g2 = g * g;
  float z = g * __builtin_fmaf(g2, c1, c0);
  float e = __builtin_amdgcn_exp2f(z);
  float r = __builtin_amdgcn_rcpf(1.0f + e);
  return __builtin_fmaf(-g, r, g);
}

__global__ __launch_bounds__(256, 2) void k2_kernel(const u16* __restrict__ dskip, char* __restrict__ ws) {
  __shared__ __attribute__((aligned(16))) char smem[K2_LDS];
  const int tid = threadIdx.x;
  const int lane = tid & 63;
  const int wave = __builtin_amdgcn_readfirstlane(tid >> 6);
  const int b = blockIdx.x;

  if (b < 256) {
    const int tt = ((b & 7) >> 2) * 8 + (b >> 5);
    const int fg = (b & 3) * 4 + ((b >> 3) & 3);
    const u16* w1 = (const u16*)(ws + WS_W1F) + (size_t)fg * 16 * 14 * 512;
    const u16* xf = (const u16*)(ws + WS_XNF) + (size_t)(tt * 8) * 16 * 512;
    f32x4 acc[14][2];
#pragma unroll
    for (int i = 0; i < 14; ++i) { acc[i][0] = (f32x4){0.f, 0.f, 0.f, 0.f}; acc[i][1] = (f32x4){0.f, 0.f, 0.f, 0.f}; }
    const u16* gsrc[6];
    int gdst[6];
    int gstep[6];
#pragma unroll
    for (int i = 0; i < 6; ++i) {
      int p = wave + 4 * i;
      if (p >= 22) p -= 22;
      if (p < 14) { gsrc[i] = w1 + p * 512 + lane * 8; gstep[i] = 14 * 512; }
      else { gsrc[i] = xf + (p - 14) * 16 * 512 + lane * 8; gstep[i] = 512; }
      gdst[i] = p * 1024 + lane * 16;
    }
    auto issue1 = [&](int ks, int i) {
      char* sb = smem + (ks % 3) * G1_STAGE;
      __builtin_amdgcn_global_load_lds((const __attribute__((address_space(1))) u32*)(gsrc[i] + ks * gstep[i]),
                                       (__attribute__((address_space(3))) u32*)(sb + gdst[i]), 16, 0, 0);
    };
#pragma unroll
    for (int i = 0; i < 6; ++i) issue1(0, i);
#pragma unroll
    for (int i = 0; i < 6; ++i) issue1(1, i);
#pragma unroll
    for (int i = 0; i < 6; ++i) issue1(2, i);
    asm volatile("s_waitcnt vmcnt(12)" ::: "memory");
    __builtin_amdgcn_s_barrier();
    bf16x8 alo[7], ahi[7], t0, t1;
    {
      const char* buf = smem + lane * 16;
      t0 = *(const bf16x8*)(buf + (14 + 2 * wave) * 1024);
      t1 = *(const bf16x8*)(buf + (15 + 2 * wave) * 1024);
#pragma unroll
      for (int t = 0; t < 7; ++t) alo[t] = *(const bf16x8*)(buf + t * 1024);
    }
#pragma unroll
    for (int ks = 0; ks < 16; ++ks) {
      const char* buf = smem + (ks % 3) * G1_STAGE + lane * 16;
#pragma unroll
      for (int t = 0; t < 7; ++t) {
        acc[t][0] = __builtin_amdgcn_mfma_f32_16x16x32_bf16(alo[t], t0, acc[t][0], 0, 0, 0);
        acc[t][1] = __builtin_amdgcn_mfma_f32_16x16x32_bf16(alo[t], t1, acc[t][1], 0, 0, 0);
        __builtin_amdgcn_sched_barrier(0);
        ahi[t] = *(const bf16x8*)(buf + (7 + t) * 1024);
        __builtin_amdgcn_sched_barrier(0);
      }
      bf16x8 n0 = t0, n1 = t1;
      if (ks < 15) {
        if (ks < 14) asm volatile("s_waitcnt vmcnt(6) lgkmcnt(0)" ::: "memory");
        else asm volatile("s_waitcnt vmcnt(0) lgkmcnt(0)" ::: "memory");
        __builtin_amdgcn_s_barrier();
        __builtin_amdgcn_sched_barrier(0);
      }
      const char* nb = smem + ((ks + 1) % 3) * G1_STAGE + lane * 16;
#pragma unroll
      for (int t = 0; t < 7; ++t) {
        acc[7 + t][0] = __builtin_amdgcn_mfma_f32_16x16x32_bf16(ahi[t], t0, acc[7 + t][0], 0, 0, 0);
        __builtin_amdgcn_sched_barrier(0);
        if (ks < 15) {
          alo[t] = *(const bf16x8*)(nb + t * 1024);
          if (t == 0) n0 = *(const bf16x8*)(nb + (14 + 2 * wave) * 1024);
          if (t == 1) n1 = *(const bf16x8*)(nb + (15 + 2 * wave) * 1024);
          __builtin_amdgcn_sched_barrier(0);
        }
        acc[7 + t][1] = __builtin_amdgcn_mfma_f32_16x16x32_bf16(ahi[t], t1, acc[7 + t][1], 0, 0, 0);
        if (t < 6 && ks + 3 < 16) {
          __builtin_amdgcn_sched_barrier(0);
          issue1(ks + 3, t);
        }
        __builtin_amdgcn_sched_barrier(0);
      }
      t0 = n0; t1 = n1;
    }
    u16* hf = (u16*)(ws + WS_HF);
    const int c = lane & 15, Q = lane >> 4;
#pragma unroll
    for (int t = 0; t < 7; ++t) {
      int ft = fg * 7 + t;
      int ks2 = ft >> 1;
      int q = 2 * (ft & 1) + (Q >> 1);
#pragma unroll
      for (int m = 0; m < 2; ++m) {
        float hv[4];
#pragma unroll
        for (int r = 0; r < 4; ++r) {
          float g = rbf(acc[t][m][r]);
          float u = rbf(acc[t + 7][m][r]);
          hv[r] = rbf(gelu_tanh(g)) * u;
        }
        int tile = tt * 8 + 2 * wave + m;
        u32x2 o = {pack2(hv[0], hv[1]), pack2(hv[2], hv[3])};
        GST2(u32x2, hf + (((size_t)tile * 56 + ks2) * 64 + (c + 16 * q)) * 8 + 4 * (Q & 1), o);
      }
    }
    return;
  }

  {
    const int chl = wave >> 1;
    const int d = (b - 256) * 2 + chl;
    const int t128 = tid & 127;
    char* cb = smem + chl * CV_CH;
    const u16* ut = (const u16*)(ws + WS_XNT) + (size_t)d * L_;
    const u16* kc = (const u16*)(ws + WS_KC) + (size_t)d * 2 * L_;
    char* kb = cb + 4 * CV_UCOPY;
    {
      u32x4 v0[3], v1[3], kv[4];
#pragma unroll
      for (int it = 0; it < 3; ++it) {
        int cch = it * 128 + t128 - 8;
        int c0 = cch < 0 ? 0 : (cch > 255 ? 255 : cch);
        int c1 = cch + 1 < 0 ? 0 : (cch + 1 > 255 ? 255 : cch + 1);
        v0[it] = *(const u32x4*)(ut + c0 * 8);
        v1[it] = *(const u32x4*)(ut + c1 * 8);
      }
#pragma unroll
      for (int it = 0; it < 4; ++it) {
        int pc = it * 128 + t128;
        kv[it] = *(const u32x4*)(kc + pc * 8);
      }
      __builtin_amdgcn_sched_barrier(0);
#pragma unroll
      for (int it = 0; it < 3; ++it) {
        int X = it * 128 + t128;
        int cch = X - 8;
        bool ok0 = (cch >= 0 && cch < 256), ok1 = (cch + 1 >= 0 && cch + 1 < 256);
        u32 w[6] = {ok0 ? v0[it][0] : 0u, ok0 ? v0[it][1] : 0u, ok0 ? v0[it][2] : 0u, ok0 ? v0[it][3] : 0u,
                    ok1 ? v1[it][0] : 0u, ok1 ? v1[it][1] : 0u};
        u32 s1[5];
#pragma unroll
        for (int e = 0; e < 5; ++e) s1[e] = __builtin_amdgcn_alignbit(w[e + 1], w[e], 16);
        if (X < 276) {
          *(u32x4*)(cb + 0 * CV_UCOPY + X * 16) = (u32x4){w[0], w[1], w[2], w[3]};
          *(u32x4*)(cb + 1 * CV_UCOPY + X * 16) = (u32x4){s1[0], s1[1], s1[2], s1[3]};
          *(u32x4*)(cb + 2 * CV_UCOPY + X * 16) = (u32x4){w[1], w[2], w[3], w[4]};
          *(u32x4*)(cb + 3 * CV_UCOPY + X * 16) = (u32x4){s1[1], s1[2], s1[3], s1[4]};
        }
      }
#pragma unroll
      for (int it = 0; it < 4; ++it) {
        int pc = it * 128 + t128;
        int arr = pc >> 8, row = (pc >> 3) & 31, ch = pc & 7;
        *(u32x4*)(kb + arr * CV_KARR + (row + 7) * CV_KROW + ch * 16) = kv[it];
      }
    }
    if (t128 < 112) {
      int arr = t128 / 56, r = t128 % 56;
      *(uint4*)(kb + arr * CV_KARR + (r >> 3) * CV_KROW + (r & 7) * 16) = make_uint4(0, 0, 0, 0);
    }
    __syncthreads();

    const int p = wave & 1;
    const int gH = p ? 2 : 3, gL = p ? 1 : 0;
    const int a = lane & 15, q = lane >> 4;
    const char* abase = cb + ((a + 1) & 3) * CV_UCOPY + (8 * q + 4 * ((a + 1) >> 2)) * 2;
    const char* bH = kb + (a >= 8 ? CV_KARR : 0) + (8 * gH + (a & 7) + 7) * CV_KROW + 16 * q;
    const char* bL = kb + (a >= 8 ? CV_KARR : 0) + (8 * gL + (a & 7) + 7) * CV_KROW + 16 * q;
    f32x4 accH[4], accL[4];
#pragma unroll
    for (int i = 0; i < 4; ++i) { accH[i] = (f32x4){0.f, 0.f, 0.f, 0.f}; accL[i] = (f32x4){0.f, 0.f, 0.f, 0.f}; }
    const int col = (a < 8) ? (8 * gH + a) : (8 * gL + a - 8);
    u32x2 uvp[4];
#pragma unroll
    for (int rt = 0; rt < 4; ++rt) {
      int t0 = 64 * col + 16 * rt + 4 * q;
      uvp[rt] = *(const u32x2*)(ut + t0);
    }
    const float dk = bf2f(dskip[d]);
    __builtin_amdgcn_sched_barrier(0);
    {
      typedef __attribute__((address_space(3))) const char* lptr;
      u32 aaddr = (u32)(size_t)(lptr)abase;
      u32 haddr = (u32)(size_t)(lptr)bH - 31 * CV_KROW;
      u32 laddr = (u32)(size_t)(lptr)bL - 31 * CV_KROW;
      if (p) conv_main<24, 16>(aaddr, haddr, laddr, accH, accL);
      else conv_main<32, 8>(aaddr, haddr, laddr, accH, accL);
    }
    u16* r1 = (u16*)(ws + WS_R1T) + (size_t)d * L_;
#pragma unroll
    for (int rt = 0; rt < 4; ++rt) {
      int t0 = 64 * col + 16 * rt + 4 * q;
      float o[4];
#pragma unroll
      for (int r = 0; r < 4; ++r) {
        float mine = (a < 8) ? accH[rt][r] : accL[rt][r];
        float give = (a < 8) ? accL[rt][r] : accH[rt][r];
        float other = __int_as_float(__builtin_amdgcn_update_dpp(0, __float_as_int(give), 0x128, 0xf, 0xf, true));
        float y = rbf(mine + other);
        float un = bf2f((u16)(uvp[rt][r >> 1] >> (16 * (r & 1))));
        o[r] = y + rbf(un * dk);
      }
      GST2(u32x2, r1 + t0, ((u32x2){pack2(o[0], o[1]), pack2(o[2], o[3])}));
    }
  }
}

__global__ __launch_bounds__(256) void k3_kernel(const u16* __restrict__ x, char* __restrict__ ws, u16* __restrict__ out) {
  __shared__ __attribute__((aligned(16))) f32x4 red[4][16][64];
  const int tid = threadIdx.x;
  const int lane = tid & 63;
  const int wave = __builtin_amdgcn_readfirstlane(tid >> 6);
  const int b = blockIdx.x;
  const int tb = (b & 7) * 4 + ((b >> 3) & 3);
  const int db = b >> 5;
  const u16* ha = (const u16*)(ws + WS_HF) + ((size_t)(tb * 4) * 56 + wave * 14) * 512 + lane * 8;
  const u16* wb = (const u16*)(ws + WS_W2F) + ((size_t)(db * 4) * 56 + wave * 14) * 512 + lane * 8;
  f32x4 acc[4][4];
#pragma unroll
  for (int m = 0; m < 4; ++m)
#pragma unroll
    for (int n = 0; n < 4; ++n) acc[m][n] = (f32x4){0.f, 0.f, 0.f, 0.f};
  constexpr int PF = 4;
  bf16x8 A[PF][4], B[PF][4];
#pragma unroll
  for (int s = 0; s < PF; ++s)
#pragma unroll
    for (int m = 0; m < 4; ++m) {
      A[s][m] = *(const bf16x8*)(ha + ((size_t)m * 56 + s) * 512);
      B[s][m] = *(const bf16x8*)(wb + ((size_t)m * 56 + s) * 512);
    }
  __builtin_amdgcn_sched_barrier(0);
#pragma unroll
  for (int s = 0; s < 14; ++s) {
    const int sl = s % PF;
#pragma unroll
    for (int m = 0; m < 4; ++m)
#pragma unroll
      for (int n = 0; n < 4; ++n)
        acc[m][n] = __builtin_amdgcn_mfma_f32_16x16x32_bf16(A[sl][m], B[sl][n], acc[m][n], 0, 0, 0);
    __builtin_amdgcn_sched_barrier(0);
    if (s + PF < 14) {
#pragma unroll
      for (int m = 0; m < 4; ++m) {
        A[sl][m] = *(const bf16x8*)(ha + ((size_t)m * 56 + s + PF) * 512);
        B[sl][m] = *(const bf16x8*)(wb + ((size_t)m * 56 + s + PF) * 512);
      }
    }
    __builtin_amdgcn_sched_barrier(0);
  }
  const int c = lane & 15, Q = lane >> 4;
  const u16* at = (const u16*)(ws + WS_R1T);
  const int tok0 = tb * 64 + wave * 16 + 4 * Q;
  u32x2 av[4];
#pragma unroll
  for (int n = 0; n < 4; ++n) av[n] = *(const u32x2*)(at + (size_t)(db * 64 + n * 16 + c) * L_ + tok0);
  const int orow = tb * 64 + wave * 16 + (lane >> 2);
  const int ocol = db * 64 + (lane & 3) * 16;
  u32x4 xv0 = *(const u32x4*)(x + (size_t)orow * D_ + ocol);
  u32x4 xv1 = *(const u32x4*)(x + (size_t)orow * D_ + ocol + 8);
#pragma unroll
  for (int m = 0; m < 4; ++m)
#pragma unroll
    for (int n = 0; n < 4; ++n) red[wave][m * 4 + n][lane] = acc[m][n];
  __syncthreads();
  float dn[4][4];
#pragma unroll
  for (int n = 0; n < 4; ++n) {
    f32x4 v = red[0][wave * 4 + n][lane];
#pragma unroll
    for (int w = 1; w < 4; ++w) {
      f32x4 t = red[w][wave * 4 + n][lane];
      v[0] += t[0]; v[1] += t[1]; v[2] += t[2]; v[3] += t[3];
    }
#pragma unroll
    for (int r = 0; r < 4; ++r) dn[n][r] = v[r];
  }
  __syncthreads();
  u32* tp = (u32*)red + wave * (16 * 68);
#pragma unroll
  for (int n = 0; n < 4; ++n)
#pragma unroll
    for (int r = 0; r < 4; ++r) {
      u32 a16 = (r & 1) ? (av[n][r >> 1] >> 16) : (av[n][r >> 1] & 0xffffu);
      tp[(4 * Q + r) * 68 + n * 16 + c] = a16 | ((u32)f2bf(dn[n][r]) << 16);
    }
  __builtin_amdgcn_s_waitcnt(0xC07F);
  __builtin_amdgcn_wave_barrier();
  {
    const u32* rp = tp + (lane >> 2) * 68 + (lane & 3) * 16;
    u32x4 o[2];
#pragma unroll
    for (int h = 0; h < 2; ++h) {
      u32x4 p0 = *(const u32x4*)(rp + h * 8), p1 = *(const u32x4*)(rp + h * 8 + 4);
      u32x4 xv = h ? xv1 : xv0;
#pragma unroll
      for (int e = 0; e < 4; ++e) {
        u32 q0 = e < 2 ? p0[2 * e] : p1[2 * e - 4], q1 = e < 2 ? p0[2 * e + 1] : p1[2 * e - 3];
        u32 r1 = pack2(lo16(xv[e]) + lo16(q0), hi16(xv[e]) + lo16(q1));
        o[h][e] = pack2(lo16(r1) + hi16(q0), hi16(r1) + hi16(q1));
      }
    }
    *(u32x4*)(out + (size_t)orow * D_ + ocol) = o[0];
    *(u32x4*)(out + (size_t)orow * D_ + ocol + 8) = o[1];
  }
}

extern "C" void kernel_launch(void* const* d_in, const int* in_sizes, int n_in, void* d_out, int out_size,
                              void* d_ws, size_t ws_size, hipStream_t stream) {
  const u16* x = (const u16*)d_in[0];
  const u16* logA = (const u16*)d_in[1];
  const u16* Aim = (const u16*)d_in[2];
  const u16* Cre = (const u16*)d_in[3];
  const u16* Cim = (const u16*)d_in[4];
  const u16* logdt = (const u16*)d_in[5];
  const u16* dskip = (const u16*)d_in[6];
  const u16* wg = (const u16*)d_in[8];
  const u16* wu = (const u16*)d_in[9];
  const u16* wd = (const u16*)d_in[10];
  char* ws = (char*)d_ws;
  k1_kernel<<<K1_GRID, 512, 0, stream>>>(x, logA, Aim, Cre, Cim, logdt, wg, wu, wd, ws);
  k2_kernel<<<512, 256, 0, stream>>>(dskip, ws);
  k3_kernel<<<256, 256, 0, stream>>>(x, ws, (u16*)d_out);
}
```
